```python
import torch

def _softmax(x, axis=-1):
    return torch.softmax(x, dim=axis)
def _gelu(x):
    return torch.nn.functional.gelu(x, approximate="tanh")
def _silu(x):
    return torch.nn.functional.silu(x)
def _sigmoid(x):
    return torch.sigmoid(x)
def _layernorm(x, axis=-1, eps=1e-5):
    m = x.mean(dim=axis, keepdim=True)
    v = x.var(dim=axis, keepdim=True, unbiased=False)
    return (x - m) / torch.sqrt(v + eps)
def _scatter_add(tgt, idx, src, axis=0):
    return tgt.clone().index_add_(axis, idx, src)
def _segment_sum(src, seg, n):
    out = torch.zeros((n,) + src.shape[1:], dtype=src.dtype)
    return out.index_add_(0, seg, src)
def _pad(x, pw):
    flat = tuple(v for pair in reversed(pw) for v in pair)
    return torch.nn.functional.pad(x, flat)

kernel_name = 'parallel_sum__linear_kernel_hedgehog__glu_gated_glu__prompt'

def setup_inputs():

    _w = (lambda a: (lambda t: t.to(torch.bfloat16) if t.is_floating_point() else t)(torch.as_tensor(a)))
    def _A_setup():
        B, S, D, H = 1, 2048, 640, 32
        Dh = D // H
        return {
            "x": _w((lambda *s: torch.randn(*s, dtype=torch.bfloat16) * 0.05)(B, S, D)),
            "wqkv": _w((lambda *s: torch.randn(*s, dtype=torch.bfloat16) * 0.4)(D, 3 * D)),
            "w_phi": _w((lambda *s: torch.randn(*s, dtype=torch.bfloat16) * 0.4)(H, Dh, Dh) * (Dh ** -0.5)),
            "wo": _w((lambda *s: torch.randn(*s, dtype=torch.bfloat16) * 0.1)(D, D)),
        }
    _A_in = _A_setup()

    def _B_setup():
        B, S, D, F = 1, 2048, 640, 1536
        return {
            "x": _w((lambda *s: torch.randn(*s, dtype=torch.bfloat16) * 0.05)(B, S, D)),
            "w_gate": _w((lambda *s: torch.randn(*s, dtype=torch.bfloat16) * 0.05)(D, F)),
            "w_up": _w((lambda *s: torch.randn(*s, dtype=torch.bfloat16) * 0.05)(D, F)),
            "w_down": _w((lambda *s: torch.randn(*s, dtype=torch.bfloat16) * 0.05)(F, D)),
        }
    _B_in = _B_setup()

    return {
        "A_x": _A_in["x"],
        "A_wqkv": _A_in["wqkv"],
        "A_w_phi": _A_in["w_phi"],
        "A_wo": _A_in["wo"],
        "B_x": _B_in["x"],
        "B_w_gate": _B_in["w_gate"],
        "B_w_up": _B_in["w_up"],
        "B_w_down": _B_in["w_down"],
    }

def reference(A_x, A_wqkv, A_w_phi, A_wo, B_x, B_w_gate, B_w_up, B_w_down):
    def _A_ref(x, wqkv, w_phi, wo):
        B, S, D = x.shape
        H = 32; Dh = D // H
        qkv = (lambda x, *d: x.permute(*d))((x @ wqkv).reshape(B, S, 3, H, Dh), 2, 0, 3, 1, 4)
        q, k, v = qkv[0], qkv[1], qkv[2]
        pq = torch.einsum("bhsd,hdf->bhsf", q, w_phi)
        pk = torch.einsum("bhsd,hdf->bhsf", k, w_phi)
        phi_q = torch.concatenate([_softmax(pq, axis=-1), _softmax(-pq, axis=-1)], axis=-1)
        phi_k = torch.concatenate([_softmax(pk, axis=-1), _softmax(-pk, axis=-1)], axis=-1)
        kv = torch.einsum("bhsf,bhsd->bhfd", phi_k, v)
        z  = torch.einsum("bhsf->bhf", phi_k)
        num = torch.einsum("bhqf,bhfd->bhqd", phi_q, kv)
        den = torch.einsum("bhqf,bhf->bhq", phi_q, z)[..., None] + 1e-6
        o = num / den
        return (lambda x, *d: x.permute(*d))(o, 0, 2, 1, 3).reshape(B, S, D) @ wo

    def _B_ref(x, w_gate, w_up, w_down):
        g = torch.einsum("bsd,df->bsf", x, w_gate)
        u = torch.einsum("bsd,df->bsf", x, w_up)
        h = _sigmoid(g) * u
        return torch.einsum("bsf,fd->bsd", h, w_down)

    x = A_x
    xn = _layernorm(x, axis=-1)
    return x + _A_ref(xn, A_wqkv, A_w_phi, A_wo) + _B_ref(xn, B_w_gate, B_w_up, B_w_down)
```

```cpp
#include <hip/hip_runtime.h>
#include <hip/hip_bf16.h>
typedef __hip_bfloat16 bf16;
constexpr int S=2048, D=640, H=32, DH=20, F=1536;
__device__ inline float b2f(bf16 v){ return __bfloat162float(v);}
__device__ inline bf16 f2b(float v){ return __float2bfloat16(v);}
__device__ inline float rb(float v){ return b2f(f2b(v)); }

__global__ void ln_kernel(const bf16* x, bf16* xn){
  int r=blockIdx.x; __shared__ float red[256];
  float s=0; for(int i=threadIdx.x;i<D;i+=256) s+=b2f(x[r*D+i]);
  red[threadIdx.x]=s; __syncthreads();
  for(int o=128;o>0;o>>=1){ if(threadIdx.x<o) red[threadIdx.x]+=red[threadIdx.x+o]; __syncthreads(); }
  float m=red[0]/D; __syncthreads();
  float v=0; for(int i=threadIdx.x;i<D;i+=256){ float d=b2f(x[r*D+i])-m; v+=d*d; }
  red[threadIdx.x]=v; __syncthreads();
  for(int o=128;o>0;o>>=1){ if(threadIdx.x<o) red[threadIdx.x]+=red[threadIdx.x+o]; __syncthreads(); }
  float var=red[0]/D; float rs=1.0f/sqrtf(var+1e-5f);
  for(int i=threadIdx.x;i<D;i+=256) xn[r*D+i]=f2b((b2f(x[r*D+i])-m)*rs);
}
__global__ void gemm_naive(const bf16* A, const bf16* B, float* C, int M,int N,int K){
  __shared__ float As[16][17], Bs[16][17];
  int tx=threadIdx.x, ty=threadIdx.y; int row=blockIdx.y*16+ty, col=blockIdx.x*16+tx;
  float acc=0;
  for(int k0=0;k0<K;k0+=16){
    As[ty][tx]=b2f(A[row*K+k0+tx]); Bs[ty][tx]=b2f(B[(k0+ty)*N+col]); __syncthreads();
    for(int k=0;k<16;k++) acc+=As[ty][k]*Bs[k][tx];
    __syncthreads();
  }
  C[row*N+col]=acc;
}
__global__ void feat_kernel(const float* qkv, const bf16* wphi, float* phi  ){
  int idx=blockIdx.x*blockDim.x+threadIdx.x; if(idx>=2*S*H) return;
  int h=idx%H, s=(idx/H)%S, w=idx/(H*S);
  float q[DH]; for(int d=0;d<DH;d++) q[d]=rb(qkv[s*3*D+w*D+h*DH+d]);
  float p[DH];
  for(int f=0;f<DH;f++){ float a=0; for(int d=0;d<DH;d++) a+=q[d]*b2f(wphi[(h*DH+d)*DH+f]); p[f]=rb(a);}
  float mx=-1e30f, mn=1e30f; for(int f=0;f<DH;f++){ mx=fmaxf(mx,p[f]); mn=fminf(mn,p[f]); }
  float s1=0,s2=0; float e1[DH],e2[DH];
  for(int f=0;f<DH;f++){ e1[f]=expf(p[f]-mx); s1+=e1[f]; e2[f]=expf(mn-p[f]); s2+=e2[f]; }
  float* o=phi+((size_t)(w*S+s)*H+h)*40;
  for(int f=0;f<DH;f++){ o[f]=rb(e1[f]/s1); o[20+f]=rb(e2[f]/s2); }
}
__global__ void kv_kernel(const float* phi, const float* qkv, float* kv){
  int h=blockIdx.x; int t=threadIdx.x; if(t>=40*21) return;
  int f=t/21, d=t%21; float a=0;
  const float* pk=phi+(size_t)S*H*40;
  for(int s=0;s<S;s++){ float v=(d<20)? rb(qkv[s*3*D+2*D+h*DH+d]) : 1.0f; a+=pk[(s*H+h)*40+f]*v; }
  kv[(h*40+f)*21+d]=a;
}
__global__ void o_kernel(const float* phi, const float* kv, bf16* o){
  int idx=blockIdx.x*blockDim.x+threadIdx.x; if(idx>=S*H) return;
  int h=idx%H, s=idx/H;
  const float* pq=phi+(size_t)(s*H+h)*40;
  float den=0; for(int f=0;f<40;f++) den+=pq[f]*kv[(h*40+f)*21+20];
  den+=1e-6f;
  for(int d=0;d<DH;d++){ float a=0; for(int f=0;f<40;f++) a+=pq[f]*kv[(h*40+f)*21+d]; o[s*D+h*DH+d]=f2b(a/den);}
}
__global__ void h_kernel(const float* g,const float* u, bf16* h){
  int i=blockIdx.x*blockDim.x+threadIdx.x; if(i>=S*F) return;
  float gg=g[i]; float sg=1.0f/(1.0f+expf(-gg)); h[i]=f2b(sg*u[i]);
}
__global__ void fin_kernel(const bf16* x,const float* a,const float* b, bf16* out){
  int i=blockIdx.x*blockDim.x+threadIdx.x; if(i>=S*D) return;
  out[i]=f2b(b2f(x[i])+a[i]+b[i]);
}
extern "C" void kernel_launch(void* const* d_in, const int* in_sizes, int n_in,
                              void* d_out, int out_size, void* d_ws, size_t ws_size,
                              hipStream_t stream) {
  const bf16* x=(const bf16*)d_in[0]; const bf16* wqkv=(const bf16*)d_in[1]; const bf16* wphi=(const bf16*)d_in[2];
  const bf16* wo=(const bf16*)d_in[3]; const bf16* wg=(const bf16*)d_in[5]; const bf16* wu=(const bf16*)d_in[6]; const bf16* wd=(const bf16*)d_in[7];
  char* ws=(char*)d_ws; size_t off=0; auto alloc=[&](size_t b){ void* p=ws+off; off+=(b+255)/256*256; return p; };
  bf16* xn=(bf16*)alloc(S*D*2); float* qkv=(float*)alloc((size_t)S*3*D*4); float* g=(float*)alloc((size_t)S*F*4); float* u=(float*)alloc((size_t)S*F*4);
  float* phi=(float*)alloc((size_t)2*S*H*40*4); float* kv=(float*)alloc(H*40*21*4); bf16* o=(bf16*)alloc(S*D*2); bf16* hh=(bf16*)alloc((size_t)S*F*2);
  float* a=(float*)alloc((size_t)S*D*4); float* b=(float*)alloc((size_t)S*D*4);
  ln_kernel<<<S,256,0,stream>>>(x,xn);
  dim3 tb(16,16);
  gemm_naive<<<dim3(3*D/16,S/16),tb,0,stream>>>(xn,wqkv,qkv,S,3*D,D);
  gemm_naive<<<dim3(F/16,S/16),tb,0,stream>>>(xn,wg,g,S,F,D);
  gemm_naive<<<dim3(F/16,S/16),tb,0,stream>>>(xn,wu,u,S,F,D);
  feat_kernel<<<(2*S*H+255)/256,256,0,stream>>>(qkv,wphi,phi);
  kv_kernel<<<H,896,0,stream>>>(phi,qkv,kv);
  o_kernel<<<(S*H+255)/256,256,0,stream>>>(phi,kv,o);
  h_kernel<<<(S*F+255)/256,256,0,stream>>>(g,u,hh);
  gemm_naive<<<dim3(D/16,S/16),tb,0,stream>>>(o,wo,a,S,D,D);
  gemm_naive<<<dim3(D/16,S/16),tb,0,stream>>>(hh,wd,b,S,D,F);
  fin_kernel<<<(S*D+255)/256,256,0,stream>>>(x,a,b,(bf16*)d_out);
}
```

```cpp
#include <hip/hip_runtime.h>
#include <stdint.h>

typedef __bf16 bf16x2 __attribute__((ext_vector_type(2)));
typedef __bf16 bf16x4 __attribute__((ext_vector_type(4)));
typedef __bf16 bf16x8 __attribute__((ext_vector_type(8)));
typedef float f32x2 __attribute__((ext_vector_type(2)));
typedef float f32x4 __attribute__((ext_vector_type(4)));
typedef float f32x8 __attribute__((ext_vector_type(8)));
typedef float f32x16 __attribute__((ext_vector_type(16)));
typedef unsigned u32x2 __attribute__((ext_vector_type(2)));
typedef unsigned u32x4 __attribute__((ext_vector_type(4)));
#define LAS __attribute__((address_space(3)))
#define GAS __attribute__((address_space(1)))
#define DI __device__ __forceinline__
typedef __amdgpu_buffer_rsrc_t rsrc_t;

constexpr int KS = 68;
constexpr size_t WS_FLAGS = 0;
constexpr size_t WS_KVP   = 32768;
constexpr int    KVP_STRIDE = 7168;
constexpr size_t WS_AFR   = 1u << 20;
constexpr size_t AFR_BYTES = (size_t)128 * KS * 1024;
constexpr size_t WS_BFR   = WS_AFR + AFR_BYTES;
constexpr size_t BFR_BYTES = (size_t)40 * KS * 1024;

constexpr float LOG2E = 1.4426950408889634f;

DI rsrc_t mk_rsrc(const void* p, unsigned bytes) { return __builtin_amdgcn_make_buffer_rsrc((void*)p, (short)0, (int)bytes, 0x00020000); }
DI unsigned rfl(unsigned v) { return __builtin_amdgcn_readfirstlane(v); }
DI void dma16(unsigned lds_addr, unsigned voff, rsrc_t r, unsigned soff) {
  asm volatile("s_mov_b32 m0, %0\n\ts_nop 0\n\tbuffer_load_dwordx4 %1, %2, %3 offen lds" ::"s"(lds_addr), "v"(voff), "s"(r), "s"(soff) : "m0", "memory");
}
#define WAIT_BAR(N) asm volatile("s_waitcnt vmcnt(" #N ") lgkmcnt(0)\n\ts_barrier" ::: "memory")
#define LDS_BAR() asm volatile("s_waitcnt lgkmcnt(0)\n\ts_barrier" ::: "memory")

DI bf16x8 ld_b128(const LAS char* p) { return *(const LAS bf16x8*)p; }
DI bf16x4 ld_tr(const LAS char* p) { return __builtin_amdgcn_ds_read_tr16_b64_v4bf16((LAS bf16x4*)p); }
DI bf16x8 cat(bf16x4 a, bf16x4 b) { return __builtin_shufflevector(a, b, 0, 1, 2, 3, 4, 5, 6, 7); }
DI f32x16 mfma32(bf16x8 a, bf16x8 b, f32x16 c) { return __builtin_amdgcn_mfma_f32_32x32x16_bf16(a, b, c, 0, 0, 0); }
DI f32x4 mfma16(bf16x8 a, bf16x8 b, f32x4 c) { return __builtin_amdgcn_mfma_f32_16x16x32_bf16(a, b, c, 0, 0, 0); }
DI float dot_sum(bf16x8 v, float acc) {
  const u32x4 w = __builtin_bit_cast(u32x4, v);
  const bf16x2 one = __builtin_bit_cast(bf16x2, 0x3f803f80u);
#pragma unroll
  for (int i = 0; i < 4; ++i) { const unsigned e = w[i]; acc = __builtin_amdgcn_fdot2_f32_bf16(__builtin_bit_cast(bf16x2, e), one, acc, false); }
  return acc;
}
DI float dot_sq(bf16x8 v, float acc) {
  const u32x4 w = __builtin_bit_cast(u32x4, v);
#pragma unroll
  for (int i = 0; i < 4; ++i) { const unsigned e = w[i]; const bf16x2 p = __builtin_bit_cast(bf16x2, e); acc = __builtin_amdgcn_fdot2_f32_bf16(p, p, acc, false); }
  return acc;
}
DI unsigned pk2(float a, float b) { bf16x2 p; p[0] = (__bf16)a; p[1] = (__bf16)b; return __builtin_bit_cast(unsigned, p); }
DI float swap_add(float v) { auto r = __builtin_amdgcn_permlane32_swap(__float_as_uint(v), __float_as_uint(v), false, false); return __uint_as_float(r[0]) + __uint_as_float(r[1]); }
DI float swap_max(float v) { auto r = __builtin_amdgcn_permlane32_swap(__float_as_uint(v), __float_as_uint(v), false, false); return fmaxf(__uint_as_float(r[0]), __uint_as_float(r[1])); }
DI float swap_min(float v) { auto r = __builtin_amdgcn_permlane32_swap(__float_as_uint(v), __float_as_uint(v), false, false); return fminf(__uint_as_float(r[0]), __uint_as_float(r[1])); }

constexpr int XS_OFF = 0, XS_SLOT = 32768;
constexpr int WS_OFF = 3 * XS_SLOT;
constexpr int WG_SLOT = 24608, WG_U = 12288 + 32;
constexpr int WQ_SLOT = 16384;
constexpr int GL_MISC = WS_OFF + 2 * WG_SLOT;
constexpr int QK_PHI = WS_OFF + 2 * WQ_SLOT;
constexpr int QK_MISC = QK_PHI + 8192;
constexpr int K1_LDS = 160 * 1024;

struct K1Args { const uint16_t *x, *wqkv, *wphi, *wo, *wg, *wu, *wd; char* ws; };

DI void relayout_issue(const K1Args& a, int unit, unsigned scratch, int lane) {
  const int ks = unit / 10, cg = unit - ks * 10;
  const rsrc_t r = ks < 20 ? mk_rsrc(a.wo, 640 * 640 * 2) : mk_rsrc(a.wd, 1536 * 640 * 2);
  const unsigned row0 = ks < 20 ? ks * 32 : (ks - 20) * 32;
  const unsigned voff = (lane >> 3) * 1280 + (lane & 7) * 16;
#pragma unroll
  for (int i = 0; i < 4; ++i) dma16(scratch + i * 1024, voff, r, (row0 + 8 * i) * 1280 + cg * 128);
}
DI void relayout_finish(const K1Args& a, int unit, const LAS char* sc, int lane) {
  const int ks = unit / 10, cg = unit - ks * 10;
  const int q = lane >> 4, i = lane & 15;
  char* dst = a.ws + WS_BFR + ((size_t)(cg * 4) * KS + ks) * 1024 + lane * 16;
#pragma unroll
  for (int c4 = 0; c4 < 4; ++c4) {
    bf16x4 v[2];
#pragma unroll
    for (int u = 0; u < 2; ++u) {
      const int rb = ks < 20 ? 8 * q + 4 * u : 16 * (q >> 1) + 4 * (q & 1) + 8 * u;
      v[u] = ld_tr(sc + (rb + (i >> 2)) * 128 + c4 * 32 + (i & 3) * 8);
    }
    *(bf16x8*)(dst + (size_t)c4 * KS * 1024) = cat(v[0], v[1]);
  }
}

DI void stage_x(const rsrc_t rx, unsigned lds0, int t, int w, const unsigned (&vx)[2]) {
  const unsigned slot = lds0 + XS_OFF + (t % 3) * XS_SLOT + w * 4096;
#pragma unroll
  for (int i = 0; i < 4; ++i) dma16(slot + i * 1024, vx[i & 1], rx, t * 128 + i * 10240);
}

DI void glu_body(const K1Args& a, char* smem, int p, int j, int w, int lane, int b) {
  const LAS char* sm = (const LAS char*)smem;
  const unsigned lds0 = (unsigned)(uintptr_t)sm;
  const int r = lane & 31, h = lane >> 5;
  const int rq = w >> 1, ch = w & 1, rot = (rq + ch) & 3;
  const rsrc_t rx = mk_rsrc(a.x + (size_t)p * 256 * 640, 256 * 1280);
  const rsrc_t rw = mk_rsrc((w < 4 ? a.wg : a.wu), 640 * 3072);
  unsigned vx[2];
#pragma unroll
  for (int e = 0; e < 2; ++e) vx[e] = (w * 32 + (lane >> 3)) * 1280 + (((lane & 7) ^ ((lane >> 4) | (4 * e))) * 16);
  unsigned vw[3];
#pragma unroll
  for (int i = 0; i < 3; ++i) { const int P = ((w & 3) * 3 + i) * 64 + lane; const int row = P / 12, c = P - row * 12; vw[i] = row * 3072 + j * 192 + c * 16; }
  const unsigned wdst = lds0 + WS_OFF + (w < 4 ? 0 : WG_U) + (w & 3) * 3072;
  auto stage_w = [&](int t) {
#pragma unroll
    for (int i = 0; i < 3; ++i) dma16(wdst + (t & 1) * WG_SLOT + i * 1024, vw[i], rw, t * (64 * 3072));
  };
  const int unit = b * 3 + w;
  const bool job = w < 3 && unit < 680;
  if (job) relayout_issue(a, unit, lds0 + XS_OFF + 2 * XS_SLOT + w * 4096, lane);
  stage_w(0); stage_x(rx, lds0, 0, w, vx); stage_w(1); stage_x(rx, lds0, 1, w, vx);
  if (job) {
    asm volatile("s_waitcnt vmcnt(14)" ::: "memory");
    relayout_finish(a, unit, sm + XS_OFF + 2 * XS_SLOT + w * 4096, lane);
  }
  unsigned xa[4], wa[4];
#pragma unroll
  for (int i = 0; i < 4; ++i) {
    const int s = (i + rot) & 3;
    xa[i] = XS_OFF + (rq * 64 + r) * 128 + (((2 * s + h) ^ ((r >> 1) & 7)) * 16);
    const int G = lane >> 4, blk = G & 1, q = (lane & 15) >> 2, pp = lane & 3;
    wa[i] = WS_OFF + blk * WG_U + (16 * s + 8 * h + q) * 192 + (48 * ch + 4 * pp) * 2;
  }
  f32x16 acc[2][3];
#pragma unroll
  for (int i = 0; i < 2; ++i)
#pragma unroll
    for (int n = 0; n < 3; ++n)
#pragma unroll
      for (int e = 0; e < 16; ++e) acc[i][n][e] = 0.f;
  float sum[2] = {0.f, 0.f}, sq[2] = {0.f, 0.f}, cs[3] = {0.f, 0.f, 0.f};
  bf16x8 XF[2][2], WF[2][3];
  auto load_frags = [&](int t, int i, int set) {
#pragma unroll
    for (int rt = 0; rt < 2; ++rt) XF[set][rt] = ld_b128(sm + xa[i] + (t % 3) * XS_SLOT + rt * 4096);
#pragma unroll
    for (int n = 0; n < 3; ++n) WF[set][n] = cat(ld_tr(sm + wa[i] + (t & 1) * WG_SLOT + n * 32), ld_tr(sm + wa[i] + (t & 1) * WG_SLOT + n * 32 + 768));
  };
  auto compute = [&](int i, int set) {
#pragma unroll
    for (int n = 0; n < 3; ++n)
#pragma unroll
      for (int rt = 0; rt < 2; ++rt) acc[rt][n] = mfma32(WF[set][n], XF[set][rt], acc[rt][n]);
    if (i == 0) {
#pragma unroll
      for (int n = 0; n < 3; ++n) cs[n] = dot_sum(WF[set][n], cs[n]);
      asm volatile("" : "+v"(cs[0]), "+v"(cs[1]), "+v"(cs[2]));
    }
    if ((i & 1) == 0) {
#pragma unroll
      for (int rt = 0; rt < 2; ++rt) { sum[rt] = dot_sum(XF[set][rt], sum[rt]); sq[rt] = dot_sq(XF[set][rt], sq[rt]); }
      asm volatile("" : "+v"(sum[0]), "+v"(sum[1]), "+v"(sq[0]), "+v"(sq[1]));
    }
  };
  WAIT_BAR(7);
  stage_x(rx, lds0, 2, w, vx);
  load_frags(0, 0, 0);
#pragma unroll
  for (int t = 0; t < 10; ++t) {
    load_frags(t, 1, 1); compute(0, 0);
    load_frags(t, 2, 0); compute(1, 1);
    load_frags(t, 3, 1); compute(2, 0);
    if (t < 9) {
      if (t == 0) { WAIT_BAR(4); }
      else if (t < 8) { WAIT_BAR(4); }
      else { WAIT_BAR(0); }
      if (t + 2 < 10) stage_w(t + 2);
      if (t + 3 < 10) stage_x(rx, lds0, t + 3, w, vx);
      load_frags(t + 1, 0, 0);
    }
    compute(3, 1);
  }
  LAS float* st = (LAS float*)(sm + GL_MISC);
  LAS float* csp = st + 1024;
  LAS float* csl = csp + 768 + w * 96;
#pragma unroll
  for (int rt = 0; rt < 2; ++rt) {
    const float s1 = swap_add(sum[rt]), s2 = swap_add(sq[rt]);
    if (h == 0) { f32x2 v = {s1, s2}; *(LAS f32x2*)(st + (ch * 256 + rq * 64 + rt * 32 + r) * 2) = v; }
  }
#pragma unroll
  for (int n = 0; n < 3; ++n) { const float c = swap_add(cs[n]); if (h == 0) csp[(rq * 6 + ch * 3 + n) * 32 + r] = c; }
  LDS_BAR();
  float ra[2], rb[2];
#pragma unroll
  for (int rt = 0; rt < 2; ++rt) {
    const f32x2 v0 = *(LAS f32x2*)(st + (rq * 64 + rt * 32 + r) * 2), v1 = *(LAS f32x2*)(st + (256 + rq * 64 + rt * 32 + r) * 2);
    const float mean = (v0[0] + v1[0]) * (1.f / 640.f);
    const float var = fmaxf((v0[1] + v1[1]) * (1.f / 640.f) - mean * mean, 0.f);
    const float rstd = rsqrtf(var + 1e-5f);
    ra[rt] = rstd; rb[rt] = -mean * rstd;
  }
  if (h == 0) {
#pragma unroll
    for (int n = 0; n < 3; ++n) {
      const int o = (ch * 3 + n) * 32 + r;
      csl[n * 32 + r] = (csp[o] + csp[192 + o]) + (csp[384 + o] + csp[576 + o]);
    }
  }
  asm volatile("s_waitcnt lgkmcnt(0)" ::: "memory");
  char* afr = a.ws + WS_AFR;
#pragma unroll
  for (int n = 0; n < 3; ++n) {
    f32x4 cg0 = *(LAS f32x4*)(csl + n * 32 + 4 * h), cg1 = *(LAS f32x4*)(csl + n * 32 + 8 + 4 * h);
    f32x4 cu0 = *(LAS f32x4*)(csl + n * 32 + 16 + 4 * h), cu1 = *(LAS f32x4*)(csl + n * 32 + 24 + 4 * h);
    const int P = j * 12 + ch * 6 + n * 2 + h;
    const int ks = 20 + (P >> 2), q = P & 3;
#pragma unroll
    for (int rt = 0; rt < 2; ++rt) {
      float hv[8];
#pragma unroll
      for (int e = 0; e < 8; ++e) {
        const float cg = e < 4 ? cg0[e & 3] : cg1[e & 3], cu = e < 4 ? cu0[e & 3] : cu1[e & 3];
        const float g = ra[rt] * acc[rt][n][e] + rb[rt] * cg;
        const float u = ra[rt] * acc[rt][n][8 + e] + rb[rt] * cu;
        const float ex = __builtin_amdgcn_exp2f(-LOG2E * g);
        hv[e] = u * __builtin_amdgcn_rcpf(1.f + ex);
      }
      const u32x4 o = {pk2(hv[0], hv[1]), pk2(hv[2], hv[3]), pk2(hv[4], hv[5]), pk2(hv[6], hv[7])};
      const int row = p * 256 + rq * 64 + rt * 32 + r;
      *(u32x4*)(afr + ((size_t)((row >> 4) * KS + ks) * 64 + q * 16 + (row & 15)) * 16) = o;
    }
  }
}

DI void dual_softmax(const f32x16& p, float (&L)[20]) {
  float mx = p[0], mn = p[0];
#pragma unroll
  for (int i = 1; i < 10; ++i) { mx = fmaxf(mx, p[i]); mn = fminf(mn, p[i]); }
  mx = swap_max(mx); mn = swap_min(mn);
  float s1 = 0.f, s2 = 0.f;
  const float a1 = -mx * LOG2E, a2 = mn * LOG2E;
#pragma unroll
  for (int i = 0; i < 10; ++i) {
    L[i] = __builtin_amdgcn_exp2f(p[i] * LOG2E + a1); s1 += L[i];
    L[10 + i] = __builtin_amdgcn_exp2f(a2 - p[i] * LOG2E); s2 += L[10 + i];
  }
  s1 = __builtin_amdgcn_rcpf(swap_add(s1)); s2 = __builtin_amdgcn_rcpf(swap_add(s2));
#pragma unroll
  for (int i = 0; i < 10; ++i) { L[i] *= s1; L[10 + i] *= s2; }
}

DI void qkv_body(const K1Args& a, char* smem, int p, int j, int w, int lane, int b) {
  const LAS char* sm = (const LAS char*)smem;
  LAS char* smw = (LAS char*)smem;
  const unsigned lds0 = (unsigned)(uintptr_t)sm;
  const int r = lane & 31, h = lane >> 5;
  const int rot = w & 3;
  const rsrc_t rx = mk_rsrc(a.x + (size_t)p * 256 * 640, 256 * 1280);
  const rsrc_t rw = mk_rsrc(a.wqkv, 640 * 3840);
  unsigned vx[2];
#pragma unroll
  for (int e = 0; e < 2; ++e) vx[e] = (w * 32 + (lane >> 3)) * 1280 + (((lane & 7) ^ ((lane >> 4) | (4 * e))) * 16);
  unsigned vw;
  { const int row = 8 * w + (lane >> 4); int pi = ((lane & 15) - 4 * (lane >> 4)) & 15; if (pi > 14) pi = 14; const int part = pi / 5, pc = pi - part * 5; vw = row * 3840 + part * 1280 + j * 80 + pc * 16; }
  auto stage_w = [&](int t) {
#pragma unroll
    for (int i = 0; i < 2; ++i) dma16(lds0 + WS_OFF + (t & 1) * WQ_SLOT + (2 * w + i) * 1024, vw, rw, t * (64 * 3840) + i * (4 * 3840));
  };
  const int unit = b * 3 + w;
  const bool job = w < 3 && unit < 680;
  if (job) relayout_issue(a, unit, lds0 + XS_OFF + 2 * XS_SLOT + w * 4096, lane);
  stage_w(0); stage_x(rx, lds0, 0, w, vx); stage_w(1); stage_x(rx, lds0, 1, w, vx);
  {
    const int head = (w >> 1) & 1, isk = w >> 2;
    const int kstep = (w >> 1) + (w & 1);
    const int cbase = isk * 40 + head * 20;
    const int fp = r;
    const int hh = (fp >> 2) & 1, ii = (fp & 3) + 4 * (fp >> 3);
    const bool fok = ii < 10;
    const int f = 10 * hh + ii;
    const uint16_t* wp = a.wphi + (size_t)(2 * j + head) * 400;
    unsigned short e[8];
#pragma unroll
    for (int x = 0; x < 8; ++x) {
      const int c = 16 * kstep + 8 * (x >> 2) + 4 * h + (x & 3);
      const int d = c - cbase;
      e[x] = (fok && d >= 0 && d < 20) ? wp[d * 20 + f] : (unsigned short)0;
    }
    u32x4 v = {e[0] | ((unsigned)e[1] << 16), e[2] | ((unsigned)e[3] << 16), e[4] | ((unsigned)e[5] << 16), e[6] | ((unsigned)e[7] << 16)};
    *(LAS u32x4*)(smw + QK_PHI + w * 1024 + lane * 16) = v;
  }
  if (job) {
    asm volatile("s_waitcnt vmcnt(0)" ::: "memory");
    relayout_finish(a, unit, sm + XS_OFF + 2 * XS_SLOT + w * 4096, lane);
  }
  unsigned xa[4], wa[4], wt[4];
#pragma unroll
  for (int i = 0; i < 4; ++i) {
    const int s = (i + rot) & 3;
    xa[i] = XS_OFF + (w * 32 + r) * 128 + (((2 * s + h) ^ ((r >> 1) & 7)) * 16);
    const int q = (lane & 15) >> 2;
    wa[i] = WS_OFF + (16 * s + 8 * h + q) * 256;
  }
  {
    const int blk = (lane >> 4) & 1, q = (lane & 15) >> 2, pp = lane & 3;
#pragma unroll
    for (int tl = 0; tl < 4; ++tl) wt[tl] = ((4 * tl + 2 * blk + (pp >> 1) + 4 * q) & 15) * 16 + (pp & 1) * 8;
  }
  f32x16 acc[4];
#pragma unroll
  for (int n = 0; n < 4; ++n)
#pragma unroll
    for (int e = 0; e < 16; ++e) acc[n][e] = 0.f;
  float sum = 0.f, sq = 0.f, cs[4] = {0.f, 0.f, 0.f, 0.f};
  bf16x8 XF[2], WF[2][4];
  auto load_frags = [&](int t, int i, int set) {
    XF[set] = ld_b128(sm + xa[i] + (t % 3) * XS_SLOT);
#pragma unroll
    for (int n = 0; n < 4; ++n) WF[set][n] = cat(ld_tr(sm + wa[i] + wt[n] + (t & 1) * WQ_SLOT), ld_tr(sm + wa[i] + wt[n] + (t & 1) * WQ_SLOT + 1024));
  };
  auto compute = [&](int i, int set) {
#pragma unroll
    for (int n = 0; n < 4; ++n) acc[n] = mfma32(WF[set][n], XF[set], acc[n]);
    if (i == 0) {
#pragma unroll
      for (int n = 0; n < 4; ++n) cs[n] = dot_sum(WF[set][n], cs[n]);
      asm volatile("" : "+v"(cs[0]), "+v"(cs[1]), "+v"(cs[2]), "+v"(cs[3]));
    }
    sum = dot_sum(XF[set], sum); sq = dot_sq(XF[set], sq);
    asm volatile("" : "+v"(sum), "+v"(sq));
  };
  WAIT_BAR(6);
  stage_x(rx, lds0, 2, w, vx);
  load_frags(0, 0, 0);
#pragma unroll
  for (int t = 0; t < 10; ++t) {
    load_frags(t, 1, 1); compute(0, 0);
    load_frags(t, 2, 0); compute(1, 1);
    load_frags(t, 3, 1); compute(2, 0);
    if (t < 9) {
      if (t < 8) { WAIT_BAR(4); } else { WAIT_BAR(0); }
      if (t + 2 < 10) stage_w(t + 2);
      if (t + 3 < 10) stage_x(rx, lds0, t + 3, w, vx);
      load_frags(t + 1, 0, 0);
    }
    compute(3, 1);
  }
  LAS float* csp = (LAS float*)(smw + QK_MISC);
  LAS float* csl = csp + 1024 + w * 128;
#pragma unroll
  for (int n = 0; n < 4; ++n) { const float c = swap_add(cs[n]); if (h == 0) csp[w * 128 + n * 32 + r] = c; }
  LDS_BAR();
  {
#pragma unroll
    for (int n = 0; n < 2; ++n) {
      const int o = n * 64 + lane;
      float c = 0.f;
#pragma unroll
      for (int k = 0; k < 8; ++k) c += csp[k * 128 + o];
      csl[o] = 0.5f * c;
    }
  }
  const float s1 = swap_add(sum), s2 = swap_add(sq);
  const float mean = s1 * (1.f / 640.f);
  const float rstd = rsqrtf(fmaxf(s2 * (1.f / 640.f) - mean * mean, 0.f) + 1e-5f);
  const float ra = rstd, rb = -mean * rstd;
  asm volatile("s_waitcnt lgkmcnt(0)" ::: "memory");
  bf16x8 X[8];
#pragma unroll
  for (int n = 0; n < 4; ++n) {
#pragma unroll
    for (int g = 0; g < 4; ++g) {
      const f32x4 c4 = *(LAS f32x4*)(csl + n * 32 + 8 * g + 4 * h);
#pragma unroll
      for (int e = 0; e < 4; ++e) acc[n][4 * g + e] = ra * acc[n][4 * g + e] + rb * c4[e];
    }
#pragma unroll
    for (int u = 0; u < 2; ++u) {
      const u32x4 v = {pk2(acc[n][8 * u], acc[n][8 * u + 1]), pk2(acc[n][8 * u + 2], acc[n][8 * u + 3]), pk2(acc[n][8 * u + 4], acc[n][8 * u + 5]), pk2(acc[n][8 * u + 6], acc[n][8 * u + 7])};
      X[2 * n + u] = __builtin_bit_cast(bf16x8, v);
    }
  }
  constexpr int IMG = 0, IROW = 240;
  constexpr int ONES = 61440;
  constexpr int KVT = 62464;
  const int myrow = w * 32 + r;
  f32x16 z16;
#pragma unroll
  for (int e = 0; e < 16; ++e) z16[e] = 0.f;
  auto phi_frag = [&](int id) { return ld_b128(sm + QK_PHI + id * 1024 + lane * 16); };
#pragma unroll
  for (int hd = 0; hd < 2; ++hd) {
    f32x16 pk = mfma32(phi_frag(4 + 2 * hd), X[2 + hd], z16);
    pk = mfma32(phi_frag(5 + 2 * hd), X[3 + hd], pk);
    float L[20];
    dual_softmax(pk, L);
#pragma unroll
    for (int g = 0; g < 5; ++g) { const u32x2 v = {pk2(L[4 * g], L[4 * g + 1]), pk2(L[4 * g + 2], L[4 * g + 3])}; *(LAS u32x2*)(smw + IMG + myrow * IROW + hd * 80 + h * 40 + g * 8) = v; }
  }
  {
    const u32x4 a5 = __builtin_bit_cast(u32x4, X[5]), a6 = __builtin_bit_cast(u32x4, X[6]), a7 = __builtin_bit_cast(u32x4, X[7]);
    LAS char* vb = smw + IMG + myrow * IROW + 160 + h * 8;
    *(LAS u32x2*)(vb + 0) = (u32x2){a5[0], a5[1]};
    *(LAS u32x2*)(vb + 16) = (u32x2){a5[2], a5[3]};
    *(LAS u32x2*)(vb + 32) = (u32x2){a6[0], a6[1]};
    *(LAS u32x2*)(vb + 48) = (u32x2){a6[2], a6[3]};
    *(LAS u32x2*)(vb + 64) = (u32x2){a7[0], a7[1]};
  }
  if (w == 0 && lane == 0) *(LAS u32x2*)(smw + ONES) = (u32x2){0x00003f80u, 0u};
  if (w == 1) { const int hd = lane >> 5; *(LAS u32x2*)(smw + KVT + (hd * 32 + r) * 96 + 40) = (u32x2){0u, 0u}; *(LAS u32x2*)(smw + KVT + (hd * 32 + r) * 96 + 88) = (u32x2){0u, 0u}; }
  LDS_BAR();
  char* kvp = a.ws + WS_KVP + (size_t)(j * 8 + p) * KVP_STRIDE;
  const rsrc_t rkv = mk_rsrc(a.ws + WS_KVP, 16 * 8 * KVP_STRIDE);
  if (w < 6) {
    const int hd = w / 3, mt = w - hd * 3;
    const int q = lane >> 4, i = lane & 15;
    f32x4 d0 = {0.f, 0.f, 0.f, 0.f}, d1 = {0.f, 0.f, 0.f, 0.f};
#pragma unroll
    for (int ks = 0; ks < 8; ++ks) {
      bf16x4 av[2], b0[2], b1[2];
#pragma unroll
      for (int u = 0; u < 2; ++u) {
        const int row = 32 * ks + 8 * q + 4 * u + (i >> 2);
        const LAS char* rp = sm + IMG + row * IROW;
        av[u] = ld_tr(rp + hd * 80 + mt * 32 + (i & 3) * 8);
        b0[u] = ld_tr(rp + 160 + hd * 40 + (i & 3) * 8);
        const LAS char* p1 = (i & 3) == 0 ? rp + 160 + hd * 40 + 32 : sm + ONES;
        b1[u] = ld_tr(p1);
      }
      const bf16x8 A = cat(av[0], av[1]);
      d0 = mfma16(A, cat(b0[0], b0[1]), d0);
      d1 = mfma16(A, cat(b1[0], b1[1]), d1);
    }
    const unsigned base = (unsigned)((j * 8 + p) * KVP_STRIDE + hd * 3360 + mt * 1344);
    if (mt < 2 || q < 2) __builtin_amdgcn_raw_buffer_store_b128(__builtin_bit_cast(u32x4, d0), rkv, base + lane * 16, 0, 16);
    const unsigned o1 = base + (mt < 2 ? 1024 : 512);
    if (i < 5 && (mt < 2 || q < 2)) __builtin_amdgcn_raw_buffer_store_b128(__builtin_bit_cast(u32x4, d1), rkv, o1 + (q * 5 + i) * 16, 0, 16);
  }
  bf16x8 Fq[2][3];
#pragma unroll
  for (int hd = 0; hd < 2; ++hd) {
    f32x16 pq = mfma32(phi_frag(2 * hd), X[hd], z16);
    pq = mfma32(phi_frag(2 * hd + 1), X[hd + 1], pq);
    float L[20];
    dual_softmax(pq, L);
    const u32x4 f0 = {pk2(L[0], L[1]), pk2(L[2], L[3]), pk2(L[4], L[5]), pk2(L[6], L[7])};
    const u32x4 f1 = {pk2(L[8], L[9]), pk2(L[10], L[11]), pk2(L[12], L[13]), pk2(L[14], L[15])};
    const u32x4 f2 = {pk2(L[16], L[17]), pk2(L[18], L[19]), 0u, 0u};
    Fq[hd][0] = __builtin_bit_cast(bf16x8, f0); Fq[hd][1] = __builtin_bit_cast(bf16x8, f1); Fq[hd][2] = __builtin_bit_cast(bf16x8, f2);
  }
  GAS unsigned* flags = (GAS unsigned*)(a.ws + WS_FLAGS);
  WAIT_BAR(0);
  if (w == 0) {
    if (lane == 0) __hip_atomic_store(flags + (j * 8 + p) * 32, 1u, __ATOMIC_RELAXED, __HIP_MEMORY_SCOPE_AGENT);
    for (unsigned spins = 0; spins < (1u << 20); ++spins) {
      const unsigned v = __hip_atomic_load(flags + (j * 8 + (lane & 7)) * 32, __ATOMIC_RELAXED, __HIP_MEMORY_SCOPE_AGENT);
      if (__all(v == 1u)) break;
      __builtin_amdgcn_s_sleep(1);
    }
  }
  asm volatile("s_barrier" ::: "memory");
  const int tid = w * 64 + lane;
  if (tid < 420) {
    const int hd = tid / 210, e = tid - hd * 210;
    f32x4 s = {0.f, 0.f, 0.f, 0.f};
    u32x4 v[8];
#pragma unroll
    for (int k = 0; k < 8; ++k) v[k] = __builtin_amdgcn_raw_buffer_load_b128(rkv, (unsigned)((j * 8 + k) * KVP_STRIDE + tid * 16), 0, 16);
#pragma unroll
    for (int k = 0; k < 8; ++k) s += __builtin_bit_cast(f32x4, v[k]);
    int mt, o;
    if (e < 84) { mt = 0; o = e; } else if (e < 168) { mt = 1; o = e - 84; } else { mt = 2; o = e - 168; }
    const int n0 = mt < 2 ? 64 : 32;
    int q, dc;
    if (o < n0) { q = o >> 4; dc = o & 15; } else { const int o2 = o - n0; q = o2 / 5; dc = 16 + (o2 - q * 5); }
    const int phi = 16 * mt + 4 * q;
    const int hh = phi >= 20 ? 1 : 0, m = phi - 20 * hh;
    const u32x2 val = {pk2(s[0], s[1]), pk2(s[2], s[3])};
    LAS char* kt = smw + KVT + hd * 32 * 96 + hh * 48 + m * 2;
    if (dc < 20) {
      int reg, half;
      if (hd == 0) { if (dc < 8) { reg = dc; half = 0; } else if (dc < 16) { reg = dc - 8; half = 1; } else { reg = dc - 8; half = 0; } }
      else { if (dc < 4) { reg = dc; half = 0; } else if (dc < 12) { reg = dc - 4; half = 1; } else { reg = dc - 8; half = 0; } }
      const int dp = (reg & 3) + 8 * (reg >> 2) + 4 * half;
      *(LAS u32x2*)(kt + dp * 96) = val;
    } else {
      *(LAS u32x2*)(kt + 24 * 96) = val; *(LAS u32x2*)(kt + 28 * 96) = val;
    }
  }
  LDS_BAR();
  float O[2][12];
#pragma unroll
  for (int hd = 0; hd < 2; ++hd) {
    f32x16 d = z16;
#pragma unroll
    for (int s = 0; s < 3; ++s) d = mfma32(ld_b128(sm + KVT + (hd * 32 + r) * 96 + h * 48 + s * 16), Fq[hd][s], d);
    const float inv = __builtin_amdgcn_rcpf(d[12] + 1e-6f);
#pragma unroll
    for (int e = 0; e < 12; ++e) O[hd][e] = d[e] * inv;
  }
  {
    char* afr = a.ws + WS_AFR;
    const int row = p * 256 + myrow;
    char* rowbase = afr + ((size_t)(row >> 4) * KS * 64 + (row & 15)) * 16;
    auto put = [&](int pi, u32x4 v) { const int s0 = 40 * j + 8 * pi; *(u32x4*)(rowbase + ((s0 >> 5) * 64 + ((s0 & 31) >> 3) * 16) * 16) = v; };
    const u32x4 first = {pk2(O[0][0], O[0][1]), pk2(O[0][2], O[0][3]), pk2(O[0][4], O[0][5]), pk2(O[0][6], O[0][7])};
    put(h, first);
    const u32x4 mid0 = {pk2(O[0][8], O[0][9]), pk2(O[0][10], O[0][11]), pk2(O[1][0], O[1][1]), pk2(O[1][2], O[1][3])};
    const u32x4 mid1 = {pk2(O[1][0], O[1][1]), pk2(O[1][2], O[1][3]), pk2(O[1][4], O[1][5]), pk2(O[1][6], O[1][7])};
    put(2 + h, h ? mid1 : mid0);
    if (h == 0) { const u32x4 last = {pk2(O[1][4], O[1][5]), pk2(O[1][6], O[1][7]), pk2(O[1][8], O[1][9]), pk2(O[1][10], O[1][11])}; put(4, last); }
  }
}

__global__ __launch_bounds__(512, 2) void k1_kernel(K1Args a) {
  extern __shared__ __attribute__((aligned(16))) char smem[];
  const int tid = threadIdx.x, lane = tid & 63, w = rfl(tid >> 6);
  const int b = blockIdx.x, xcd = b & 7, idx = b >> 3;
  const int p = idx & 7, j = xcd * 2 + ((idx >> 3) & 1);
  if (idx < 16) qkv_body(a, smem, p, j, w, lane, b);
  else glu_body(a, smem, p, j, w, lane, b);
}

constexpr int K2_LDS = 4 * 64 * 84 * 4;
__global__ __launch_bounds__(256, 1) void k2_kernel(const char* __restrict__ ws, const uint16_t* __restrict__ x, uint16_t* __restrict__ out) {
  extern __shared__ __attribute__((aligned(16))) char smem[];
  const int tid = threadIdx.x, lane = tid & 63, w = rfl(tid >> 6);
  const int b = blockIdx.x, xcd = b & 7, idx = b >> 3;
  const int rt64 = xcd * 4 + (idx >> 3), ct80 = idx & 7;
  const rsrc_t ra = mk_rsrc(ws + WS_AFR + (size_t)rt64 * 4 * KS * 1024, 4 * KS * 1024);
  const rsrc_t rb = mk_rsrc(ws + WS_BFR + (size_t)ct80 * 5 * KS * 1024, 5 * KS * 1024);
  constexpr int DEPTH = 4, NS = 17;
  u32x4 A[DEPTH][4], B[DEPTH][5];
  const unsigned voff = lane * 16, sbase = w * NS * 1024;
  auto load = [&](int s, int st) {
#pragma unroll
    for (int c = 0; c < 5; ++c) B[st][c] = __builtin_amdgcn_raw_buffer_load_b128(rb, voff, sbase + (c * KS + s) * 1024, 0);
#pragma unroll
    for (int r = 0; r < 4; ++r) A[st][r] = __builtin_amdgcn_raw_buffer_load_b128(ra, voff, sbase + (r * KS + s) * 1024, 0);
  };
#pragma unroll
  for (int s = 0; s < DEPTH; ++s) load(s, s);
  u32x4 xr[3];
#pragma unroll
  for (int i = 0; i < 3; ++i) {
    const int it = tid + 256 * i;
    if (it < 640) { const int row = it / 10, g = it - row * 10; xr[i] = *(const u32x4*)(x + (size_t)(rt64 * 64 + row) * 640 + ct80 * 80 + g * 8); }
  }
  f32x4 acc[5][4];
#pragma unroll
  for (int c = 0; c < 5; ++c)
#pragma unroll
    for (int r = 0; r < 4; ++r) acc[c][r] = (f32x4){0.f, 0.f, 0.f, 0.f};
#pragma unroll
  for (int s = 0; s < NS; ++s) {
    const int st = s % DEPTH;
#pragma unroll
    for (int c = 0; c < 5; ++c)
#pragma unroll
      for (int r = 0; r < 4; ++r) acc[c][r] = mfma16(__builtin_bit_cast(bf16x8, B[st][c]), __builtin_bit_cast(bf16x8, A[st][r]), acc[c][r]);
    __builtin_amdgcn_sched_barrier(0);
    if (s + DEPTH < NS) load(s + DEPTH, st);
    __builtin_amdgcn_sched_barrier(0);
  }
  LAS float* red = (LAS float*)smem;
#pragma unroll
  for (int c = 0; c < 5; ++c)
#pragma unroll
    for (int r = 0; r < 4; ++r) *(LAS f32x4*)(red + (w * 64 + r * 16 + (lane & 15)) * 84 + c * 16 + (lane >> 4) * 4) = acc[c][r];
  __syncthreads();
#pragma unroll
  for (int i = 0; i < 3; ++i) {
    const int it = tid + 256 * i;
    if (it < 640) {
      const int row = it / 10, g = it - row * 10;
      f32x4 s0 = {0.f, 0.f, 0.f, 0.f}, s1 = {0.f, 0.f, 0.f, 0.f};
#pragma unroll
      for (int k = 0; k < 4; ++k) { s0 += *(LAS f32x4*)(red + (k * 64 + row) * 84 + g * 8); s1 += *(LAS f32x4*)(red + (k * 64 + row) * 84 + g * 8 + 4); }
      u32x4 o;
#pragma unroll
      for (int e = 0; e < 4; ++e) {
        const float lo = __uint_as_float(xr[i][e] << 16), hi = __uint_as_float(xr[i][e] & 0xffff0000u);
        const float v0 = e < 2 ? s0[2 * e] : s1[2 * e - 4], v1 = e < 2 ? s0[2 * e + 1] : s1[2 * e - 3];
        o[e] = pk2(lo + v0, hi + v1);
      }
      *(u32x4*)(out + (size_t)(rt64 * 64 + row) * 640 + ct80 * 80 + g * 8) = o;
    }
  }
}

extern "C" void kernel_launch(void* const* d_in, const int* in_sizes, int n_in, void* d_out, int out_size, void* d_ws, size_t ws_size, hipStream_t stream) {
  (void)in_sizes; (void)n_in; (void)out_size; (void)ws_size;
  K1Args a;
  a.x = (const uint16_t*)d_in[0]; a.wqkv = (const uint16_t*)d_in[1]; a.wphi = (const uint16_t*)d_in[2]; a.wo = (const uint16_t*)d_in[3];
  a.wg = (const uint16_t*)d_in[5]; a.wu = (const uint16_t*)d_in[6]; a.wd = (const uint16_t*)d_in[7]; a.ws = (char*)d_ws;
  hipFuncSetAttribute((const void*)k1_kernel, hipFuncAttributeMaxDynamicSharedMemorySize, K1_LDS);
  hipFuncSetAttribute((const void*)k2_kernel, hipFuncAttributeMaxDynamicSharedMemorySize, K2_LDS);
  hipMemsetAsync((char*)d_ws + WS_FLAGS, 0, 16384, stream);
  k1_kernel<<<256, 512, K1_LDS, stream>>>(a);
  k2_kernel<<<256, 256, K2_LDS, stream>>>((const char*)d_ws, a.x, (uint16_t*)d_out);
}
```

```cpp
#include <hip/hip_runtime.h>
#include <stdint.h>

typedef __bf16 bf16x2 __attribute__((ext_vector_type(2)));
typedef __bf16 bf16x4 __attribute__((ext_vector_type(4)));
typedef __bf16 bf16x8 __attribute__((ext_vector_type(8)));
typedef float f32x2 __attribute__((ext_vector_type(2)));
typedef float f32x4 __attribute__((ext_vector_type(4)));
typedef float f32x8 __attribute__((ext_vector_type(8)));
typedef float f32x16 __attribute__((ext_vector_type(16)));
typedef unsigned u32x2 __attribute__((ext_vector_type(2)));
typedef unsigned u32x4 __attribute__((ext_vector_type(4)));
#define LAS __attribute__((address_space(3)))
#define GAS __attribute__((address_space(1)))
#define DI __device__ __forceinline__
typedef __amdgpu_buffer_rsrc_t rsrc_t;

constexpr int KS = 68;
constexpr size_t WS_EPOCH = 0;
constexpr size_t WS_KVFLAG = 4096;
constexpr size_t WS_DONE  = 16384;
constexpr size_t WS_KVP   = 32768;
constexpr int    KVP_STRIDE = 7168;
constexpr size_t WS_AFR   = 1u << 20;
constexpr size_t AFR_BYTES = (size_t)128 * KS * 1024;
constexpr size_t WS_BFR   = WS_AFR + AFR_BYTES;
constexpr size_t BFR_BYTES = (size_t)40 * KS * 1024;

constexpr float LOG2E = 1.4426950408889634f;

DI rsrc_t mk_rsrc(const void* p, unsigned bytes) { return __builtin_amdgcn_make_buffer_rsrc((void*)p, (short)0, (int)bytes, 0x00020000); }
DI unsigned rfl(unsigned v) { return __builtin_amdgcn_readfirstlane(v); }
DI void dma16(unsigned lds_addr, unsigned voff, rsrc_t r, unsigned soff) {
  asm volatile("s_mov_b32 m0, %0\n\ts_nop 0\n\tbuffer_load_dwordx4 %1, %2, %3 offen lds" ::"s"(lds_addr), "v"(voff), "s"(r), "s"(soff) : "m0", "memory");
}
#define WAIT_BAR(N) asm volatile("s_waitcnt vmcnt(" #N ") lgkmcnt(0)\n\ts_barrier" ::: "memory")
#define SB() __builtin_amdgcn_sched_barrier(0)
#define PIN1(x) asm volatile("" : "+v"(x))
#define PIN2(x, y) asm volatile("" : "+v"(x), "+v"(y))
#define LDS_BAR() asm volatile("s_waitcnt lgkmcnt(0)\n\ts_barrier" ::: "memory")

DI bf16x8 ld_b128(const LAS char* p) { return *(const LAS bf16x8*)p; }
DI bf16x4 ld_tr(const LAS char* p) { return __builtin_amdgcn_ds_read_tr16_b64_v4bf16((LAS bf16x4*)p); }
DI bf16x8 cat(bf16x4 a, bf16x4 b) { return __builtin_shufflevector(a, b, 0, 1, 2, 3, 4, 5, 6, 7); }
DI f32x16 mfma32(bf16x8 a, bf16x8 b, f32x16 c) { return __builtin_amdgcn_mfma_f32_32x32x16_bf16(a, b, c, 0, 0, 0); }
DI f32x4 mfma16(bf16x8 a, bf16x8 b, f32x4 c) { return __builtin_amdgcn_mfma_f32_16x16x32_bf16(a, b, c, 0, 0, 0); }
DI float dot_sum(bf16x8 v, float acc) {
  const u32x4 w = __builtin_bit_cast(u32x4, v);
  const bf16x2 one = __builtin_bit_cast(bf16x2, 0x3f803f80u);
#pragma unroll
  for (int i = 0; i < 4; ++i) { const unsigned e = w[i]; acc = __builtin_amdgcn_fdot2_f32_bf16(__builtin_bit_cast(bf16x2, e), one, acc, false); }
  return acc;
}
DI float dot_sq(bf16x8 v, float acc) {
  const u32x4 w = __builtin_bit_cast(u32x4, v);
#pragma unroll
  for (int i = 0; i < 4; ++i) { const unsigned e = w[i]; const bf16x2 p = __builtin_bit_cast(bf16x2, e); acc = __builtin_amdgcn_fdot2_f32_bf16(p, p, acc, false); }
  return acc;
}
DI unsigned pk2(float a, float b) { bf16x2 p; p[0] = (__bf16)a; p[1] = (__bf16)b; return __builtin_bit_cast(unsigned, p); }
DI float swap_add(float v) { auto r = __builtin_amdgcn_permlane32_swap(__float_as_uint(v), __float_as_uint(v), false, false); return __uint_as_float(r[0]) + __uint_as_float(r[1]); }
DI float swap_max(float v) { auto r = __builtin_amdgcn_permlane32_swap(__float_as_uint(v), __float_as_uint(v), false, false); return fmaxf(__uint_as_float(r[0]), __uint_as_float(r[1])); }
DI float swap_min(float v) { auto r = __builtin_amdgcn_permlane32_swap(__float_as_uint(v), __float_as_uint(v), false, false); return fminf(__uint_as_float(r[0]), __uint_as_float(r[1])); }

constexpr int XS_OFF = 0, XS_SLOT = 32768;
constexpr int WS_OFF = 3 * XS_SLOT;
constexpr int WG_SLOT = 24608, WG_U = 12288 + 32;
constexpr int WQ_SLOT = 16384;
constexpr int GL_MISC = WS_OFF + 2 * WG_SLOT;
constexpr int QK_PHI = WS_OFF + 2 * WQ_SLOT;
constexpr int QK_RAW = QK_PHI + 8192;
constexpr int QK_MISC = QK_RAW + 2048;
constexpr int K1_LDS = 160 * 1024;

#define STAMP(ws, kern, id) do {} while (0)
struct K1Args { const uint16_t *x, *wqkv, *wphi, *wo, *wg, *wu, *wd; char* ws; uint16_t* out; };
#define RLX_AGENT __ATOMIC_RELAXED, __HIP_MEMORY_SCOPE_AGENT
constexpr int SC1 = 16;
constexpr unsigned SPIN_CAP = 1u << 18;

DI void relayout_issue(const K1Args& a, int unit, unsigned scratch, int lane) {
  const int ks = unit / 10, cg = unit - ks * 10;
  const rsrc_t r = ks < 20 ? mk_rsrc(a.wo, 640 * 640 * 2) : mk_rsrc(a.wd, 1536 * 640 * 2);
  const unsigned row0 = ks < 20 ? ks * 32 : (ks - 20) * 32;
  const unsigned voff = (lane >> 3) * 1280 + (lane & 7) * 16;
#pragma unroll
  for (int i = 0; i < 4; ++i) dma16(scratch + i * 1024, voff, r, (row0 + 8 * i) * 1280 + cg * 128);
}
DI void relayout_finish(const K1Args& a, int unit, const LAS char* sc, int lane) {
  const int ks = unit / 10, cg = unit - ks * 10;
  const int q = lane >> 4, i = lane & 15;
  const rsrc_t rb = mk_rsrc(a.ws + WS_BFR, (unsigned)BFR_BYTES);
  const unsigned dst = ((cg * 4) * KS + ks) * 1024 + lane * 16;
#pragma unroll
  for (int c4 = 0; c4 < 4; ++c4) {
    bf16x4 v[2];
#pragma unroll
    for (int u = 0; u < 2; ++u) {
      const int rb = ks < 20 ? 8 * q + 4 * u : 16 * (q >> 1) + 4 * (q & 1) + 8 * u;
      v[u] = ld_tr(sc + (rb + (i >> 2)) * 128 + c4 * 32 + (i & 3) * 8);
    }
    __builtin_amdgcn_raw_buffer_store_b128(__builtin_bit_cast(u32x4, cat(v[0], v[1])), rb, dst, c4 * KS * 1024, SC1);
  }
}

DI int rot10(int t, int off) { const int v = t + off; return v >= 10 ? v - 10 : v; }
DI void stage_x(const rsrc_t rx, unsigned lds0, int t, int off, int w, const unsigned (&vx)[2]) {
  const unsigned slot = lds0 + XS_OFF + (t % 3) * XS_SLOT + w * 4096;
  const unsigned kt = rot10(t, off);
#pragma unroll
  for (int i = 0; i < 4; ++i) dma16(slot + i * 1024, vx[i & 1], rx, kt * 128 + i * 10240);
}

DI void glu_body(const K1Args& a, char* smem, int p, int j, int w, int lane, int b, int off) {
  const LAS char* sm = (const LAS char*)smem;
  const unsigned lds0 = (unsigned)(uintptr_t)sm;
  const int r = lane & 31, h = lane >> 5;
  const int rq = w >> 1, ch = w & 1, rot = (rq + ch) & 3;
  const rsrc_t rx = mk_rsrc(a.x + (size_t)p * 256 * 640, 256 * 1280);
  const rsrc_t rw = mk_rsrc((w < 4 ? a.wg : a.wu), 640 * 3072);
  unsigned vx[2];
#pragma unroll
  for (int e = 0; e < 2; ++e) vx[e] = (w * 32 + (lane >> 3)) * 1280 + (((lane & 7) ^ ((lane >> 4) | (4 * e))) * 16);
  unsigned vw[3];
#pragma unroll
  for (int i = 0; i < 3; ++i) { const int P = ((w & 3) * 3 + i) * 64 + lane; const int row = P / 12, c = P - row * 12; vw[i] = row * 3072 + j * 192 + c * 16; }
  const unsigned wdst = lds0 + WS_OFF + (w < 4 ? 0 : WG_U) + (w & 3) * 3072;
  auto stage_w = [&](int t) {
#pragma unroll
    for (int i = 0; i < 3; ++i) dma16(wdst + (t & 1) * WG_SLOT + i * 1024, vw[i], rw, rot10(t, off) * (64 * 3072));
  };
  const int unit = b * 3 + w;
  const bool job = w < 3 && unit < 680;
  if (job) relayout_issue(a, unit, lds0 + XS_OFF + 2 * XS_SLOT + w * 4096, lane);
  stage_w(0); stage_x(rx, lds0, 0, off, w, vx); stage_w(1); stage_x(rx, lds0, 1, off, w, vx);
  if (job) {
    asm volatile("s_waitcnt vmcnt(14)" ::: "memory");
    relayout_finish(a, unit, sm + XS_OFF + 2 * XS_SLOT + w * 4096, lane);
  }
  unsigned xa[4], wa[4];
#pragma unroll
  for (int i = 0; i < 4; ++i) {
    const int s = (i + rot) & 3;
    xa[i] = XS_OFF + (rq * 64 + r) * 128 + (((2 * s + h) ^ ((r >> 1) & 7)) * 16);
    const int G = lane >> 4, blk = G & 1, q = (lane & 15) >> 2, pp = lane & 3;
    wa[i] = WS_OFF + blk * WG_U + (16 * s + 8 * h + q) * 192 + (48 * ch + 4 * pp) * 2;
  }
  f32x16 acc[2][3];
#pragma unroll
  for (int i = 0; i < 2; ++i)
#pragma unroll
    for (int n = 0; n < 3; ++n)
#pragma unroll
      for (int e = 0; e < 16; ++e) acc[i][n][e] = 0.f;
  float sum[2] = {0.f, 0.f}, sq[2] = {0.f, 0.f}, cs[3] = {0.f, 0.f, 0.f};
  bf16x8 XF[2][2], WF[2][3];
  auto rdX = [&](int t, int i, int set, int rt) { XF[set][rt] = ld_b128(sm + xa[i] + (t % 3) * XS_SLOT + rt * 4096); };
  auto rdW = [&](int t, int i, int set, int n) { WF[set][n] = cat(ld_tr(sm + wa[i] + (t & 1) * WG_SLOT + n * 32), ld_tr(sm + wa[i] + (t & 1) * WG_SLOT + n * 32 + 768)); };
  auto dma_piece = [&](int tb, int k) {
    if (k < 3) { if (tb + 2 < 10) dma16(wdst + (tb & 1) * WG_SLOT + k * 1024, vw[k], rw, rot10(tb + 2, off) * (64 * 3072)); }
    else if (tb + 3 < 10) { const int i = k - 3; dma16(lds0 + XS_OFF + (tb % 3) * XS_SLOT + w * 4096 + i * 1024, vx[i & 1], rx, rot10(tb + 3, off) * 128 + i * 10240); }
  };
  auto step = [&](int i, int set, int tn, int in, bool rd, int tb, int ka, int kb) {
    const int ns = set ^ 1;
    acc[0][0] = mfma32(WF[set][0], XF[set][0], acc[0][0]);
    if (rd) { rdX(tn, in, ns, 0); rdX(tn, in, ns, 1); }
    SB();
    acc[1][0] = mfma32(WF[set][0], XF[set][1], acc[1][0]);
    if (rd) rdW(tn, in, ns, 0);
    if ((i & 1) == 0) { sum[0] = dot_sum(XF[set][0], sum[0]); sq[0] = dot_sq(XF[set][0], sq[0]); PIN2(sum[0], sq[0]); }
    if (i == 1) { cs[0] = dot_sum(WF[set][0], cs[0]); PIN1(cs[0]); }
    SB();
    acc[0][1] = mfma32(WF[set][1], XF[set][0], acc[0][1]);
    if (ka >= 0) dma_piece(tb, ka);
    SB();
    acc[1][1] = mfma32(WF[set][1], XF[set][1], acc[1][1]);
    if (rd) rdW(tn, in, ns, 1);
    if ((i & 1) == 0) { sum[1] = dot_sum(XF[set][1], sum[1]); sq[1] = dot_sq(XF[set][1], sq[1]); PIN2(sum[1], sq[1]); }
    if (i == 1) { cs[1] = dot_sum(WF[set][1], cs[1]); PIN1(cs[1]); }
    SB();
    acc[0][2] = mfma32(WF[set][2], XF[set][0], acc[0][2]);
    if (kb >= 0) dma_piece(tb, kb);
    SB();
    acc[1][2] = mfma32(WF[set][2], XF[set][1], acc[1][2]);
    if (rd) rdW(tn, in, ns, 2);
    if (i == 1) { cs[2] = dot_sum(WF[set][2], cs[2]); PIN1(cs[2]); }
    SB();
  };
  WAIT_BAR(7);
  STAMP(a.ws, 0, 1);
  stage_x(rx, lds0, 2, off, w, vx);
  rdX(0, 0, 0, 0); rdX(0, 0, 0, 1); rdW(0, 0, 0, 0); rdW(0, 0, 0, 1); rdW(0, 0, 0, 2);
  SB();
#pragma unroll
  for (int t = 0; t < 10; ++t) {
    const int tb = t - 1;
    step(0, 0, t, 1, true, tb, t > 0 ? 2 : -1, t > 0 ? 3 : -1);
    step(1, 1, t, 2, true, tb, t > 0 ? 4 : -1, t > 0 ? 5 : -1);
    step(2, 0, t, 3, true, tb, t > 0 ? 6 : -1, -1);
    if (t < 9) {
      if (t < 8) { WAIT_BAR(4); } else { WAIT_BAR(0); }
      SB();
      step(3, 1, t + 1, 0, true, t, 0, 1);
    } else step(3, 1, 0, 0, false, 0, -1, -1);
  }
  STAMP(a.ws, 0, 2);
  LAS float* st = (LAS float*)(sm + GL_MISC);
  LAS float* csp = st + 1024;
  LAS float* csl = csp + 768 + w * 96;
#pragma unroll
  for (int rt = 0; rt < 2; ++rt) {
    const float s1 = swap_add(sum[rt]), s2 = swap_add(sq[rt]);
    if (h == 0) { f32x2 v = {s1, s2}; *(LAS f32x2*)(st + (ch * 256 + rq * 64 + rt * 32 + r) * 2) = v; }
  }
#pragma unroll
  for (int n = 0; n < 3; ++n) { const float c = swap_add(cs[n]); if (h == 0) csp[(rq * 6 + ch * 3 + n) * 32 + r] = c; }
  LDS_BAR();
  float ra[2], rb[2];
#pragma unroll
  for (int rt = 0; rt < 2; ++rt) {
    const f32x2 v0 = *(LAS f32x2*)(st + (rq * 64 + rt * 32 + r) * 2), v1 = *(LAS f32x2*)(st + (256 + rq * 64 + rt * 32 + r) * 2);
    const float mean = (v0[0] + v1[0]) * (1.f / 640.f);
    const float var = fmaxf((v0[1] + v1[1]) * (1.f / 640.f) - mean * mean, 0.f);
    const float rstd = rsqrtf(var + 1e-5f);
    ra[rt] = rstd; rb[rt] = -mean * rstd;
  }
  if (h == 0) {
#pragma unroll
    for (int n = 0; n < 3; ++n) {
      const int o = (ch * 3 + n) * 32 + r;
      csl[n * 32 + r] = (csp[o] + csp[192 + o]) + (csp[384 + o] + csp[576 + o]);
    }
  }
  asm volatile("s_waitcnt lgkmcnt(0)" ::: "memory");
  const rsrc_t rafr = mk_rsrc(a.ws + WS_AFR, (unsigned)AFR_BYTES);
#pragma unroll
  for (int n = 0; n < 3; ++n) {
    f32x4 cg0 = *(LAS f32x4*)(csl + n * 32 + 4 * h), cg1 = *(LAS f32x4*)(csl + n * 32 + 8 + 4 * h);
    f32x4 cu0 = *(LAS f32x4*)(csl + n * 32 + 16 + 4 * h), cu1 = *(LAS f32x4*)(csl + n * 32 + 24 + 4 * h);
    const int P = j * 12 + ch * 6 + n * 2 + h;
    const int ks = 20 + (P >> 2), q = P & 3;
#pragma unroll
    for (int rt = 0; rt < 2; ++rt) {
      float hv[8];
#pragma unroll
      for (int e = 0; e < 8; ++e) {
        const float cg = e < 4 ? cg0[e & 3] : cg1[e & 3], cu = e < 4 ? cu0[e & 3] : cu1[e & 3];
        const float g = ra[rt] * acc[rt][n][e] + rb[rt] * cg;
        const float u = ra[rt] * acc[rt][n][8 + e] + rb[rt] * cu;
        const float ex = __builtin_amdgcn_exp2f(-LOG2E * g);
        hv[e] = u * __builtin_amdgcn_rcpf(1.f + ex);
      }
      const u32x4 o = {pk2(hv[0], hv[1]), pk2(hv[2], hv[3]), pk2(hv[4], hv[5]), pk2(hv[6], hv[7])};
      const int row = p * 256 + rq * 64 + rt * 32 + r;
      __builtin_amdgcn_raw_buffer_store_b128(o, rafr, (unsigned)((((row >> 4) * KS + ks) * 64 + q * 16 + (row & 15)) * 16), 0, SC1);
    }
  }
  STAMP(a.ws, 0, 7);
}

DI void dual_softmax(const f32x16& p, float (&L)[20]) {
  float mx = p[0], mn = p[0];
#pragma unroll
  for (int i = 1; i < 10; ++i) { mx = fmaxf(mx, p[i]); mn = fminf(mn, p[i]); }
  mx = swap_max(mx); mn = swap_min(mn);
  float s1 = 0.f, s2 = 0.f;
  const float a1 = -mx * LOG2E, a2 = mn * LOG2E;
#pragma unroll
  for (int i = 0; i < 10; ++i) {
    L[i] = __builtin_amdgcn_exp2f(p[i] * LOG2E + a1); s1 += L[i];
    L[10 + i] = __builtin_amdgcn_exp2f(a2 - p[i] * LOG2E); s2 += L[10 + i];
  }
  s1 = __builtin_amdgcn_rcpf(swap_add(s1)); s2 = __builtin_amdgcn_rcpf(swap_add(s2));
#pragma unroll
  for (int i = 0; i < 10; ++i) { L[i] *= s1; L[10 + i] *= s2; }
}

DI void qkv_body(const K1Args& a, char* smem, int p, int j, int w, int lane, int b, int off, unsigned tag) {
  const LAS char* sm = (const LAS char*)smem;
  LAS char* smw = (LAS char*)smem;
  const unsigned lds0 = (unsigned)(uintptr_t)sm;
  const int r = lane & 31, h = lane >> 5;
  const int rot = w & 3;
  const rsrc_t rx = mk_rsrc(a.x + (size_t)p * 256 * 640, 256 * 1280);
  const rsrc_t rw = mk_rsrc(a.wqkv, 640 * 3840);
  unsigned vx[2];
#pragma unroll
  for (int e = 0; e < 2; ++e) vx[e] = (w * 32 + (lane >> 3)) * 1280 + (((lane & 7) ^ ((lane >> 4) | (4 * e))) * 16);
  unsigned vw;
  { const int row = 8 * w + (lane >> 4); int pi = ((lane & 15) - 4 * (lane >> 4)) & 15; if (pi > 14) pi = 14; const int part = pi / 5, pc = pi - part * 5; vw = row * 3840 + part * 1280 + j * 80 + pc * 16; }
  auto stage_w = [&](int t) {
#pragma unroll
    for (int i = 0; i < 2; ++i) dma16(lds0 + WS_OFF + (t & 1) * WQ_SLOT + (2 * w + i) * 1024, vw, rw, rot10(t, off) * (64 * 3840) + i * (4 * 3840));
  };
  const int unit = b * 3 + w;
  const bool job = w < 3 && unit < 680;
  if (job) relayout_issue(a, unit, lds0 + XS_OFF + 2 * XS_SLOT + w * 4096, lane);
  if (w == 7) {
    const rsrc_t rp = mk_rsrc(a.wphi + j * 800, 1600);
    dma16(lds0 + QK_RAW, lane * 16, rp, 0); dma16(lds0 + QK_RAW + 1024, lane * 16, rp, 1024);
  }
  stage_w(0); stage_x(rx, lds0, 0, off, w, vx); stage_w(1); stage_x(rx, lds0, 1, off, w, vx);
  if (job) {
    asm volatile("s_waitcnt vmcnt(12)" ::: "memory");
    relayout_finish(a, unit, sm + XS_OFF + 2 * XS_SLOT + w * 4096, lane);
  }
  unsigned xa[4], wa[4], wt[4];
#pragma unroll
  for (int i = 0; i < 4; ++i) {
    const int s = (i + rot) & 3;
    xa[i] = XS_OFF + (w * 32 + r) * 128 + (((2 * s + h) ^ ((r >> 1) & 7)) * 16);
    const int q = (lane & 15) >> 2;
    wa[i] = WS_OFF + (16 * s + 8 * h + q) * 256;
  }
  {
    const int blk = (lane >> 4) & 1, q = (lane & 15) >> 2, pp = lane & 3;
#pragma unroll
    for (int tl = 0; tl < 4; ++tl) wt[tl] = ((4 * tl + 2 * blk + (pp >> 1) + 4 * q) & 15) * 16 + (pp & 1) * 8;
  }
  f32x16 acc[4];
#pragma unroll
  for (int n = 0; n < 4; ++n)
#pragma unroll
    for (int e = 0; e < 16; ++e) acc[n][e] = 0.f;
  float sum = 0.f, sq = 0.f, cs[4] = {0.f, 0.f, 0.f, 0.f};
  bf16x8 XF[2], WF[2][4];
  auto rdX = [&](int t, int i, int set) { XF[set] = ld_b128(sm + xa[i] + (t % 3) * XS_SLOT); };
  auto rdW = [&](int t, int i, int set, int n) { WF[set][n] = cat(ld_tr(sm + wa[i] + wt[n] + (t & 1) * WQ_SLOT), ld_tr(sm + wa[i] + wt[n] + (t & 1) * WQ_SLOT + 1024)); };
  auto dma_piece = [&](int tb, int k) {
    if (k < 2) { if (tb + 2 < 10) dma16(lds0 + WS_OFF + (tb & 1) * WQ_SLOT + (2 * w + k) * 1024, vw, rw, rot10(tb + 2, off) * (64 * 3840) + k * (4 * 3840)); }
    else if (tb + 3 < 10) { const int i = k - 2; dma16(lds0 + XS_OFF + (tb % 3) * XS_SLOT + w * 4096 + i * 1024, vx[i & 1], rx, rot10(tb + 3, off) * 128 + i * 10240); }
  };
  auto step = [&](int i, int set, int tn, int in, bool rd, int tb, int ka, int kb) {
    const int ns = set ^ 1;
    acc[0] = mfma32(WF[set][0], XF[set], acc[0]);
    if (rd) { rdX(tn, in, ns); rdW(tn, in, ns, 0); }
    if (i == 1) { cs[0] = dot_sum(WF[set][0], cs[0]); PIN1(cs[0]); }
    SB();
    acc[1] = mfma32(WF[set][1], XF[set], acc[1]);
    if (rd) rdW(tn, in, ns, 1);
    if (ka >= 0) dma_piece(tb, ka);
    sum = dot_sum(XF[set], sum); PIN1(sum);
    if (i == 1) { cs[1] = dot_sum(WF[set][1], cs[1]); PIN1(cs[1]); }
    SB();
    acc[2] = mfma32(WF[set][2], XF[set], acc[2]);
    if (rd) rdW(tn, in, ns, 2);
    sq = dot_sq(XF[set], sq); PIN1(sq);
    if (i == 1) { cs[2] = dot_sum(WF[set][2], cs[2]); PIN1(cs[2]); }
    SB();
    acc[3] = mfma32(WF[set][3], XF[set], acc[3]);
    if (rd) rdW(tn, in, ns, 3);
    if (kb >= 0) dma_piece(tb, kb);
    if (i == 1) { cs[3] = dot_sum(WF[set][3], cs[3]); PIN1(cs[3]); }
    SB();
  };
  WAIT_BAR(6);
  STAMP(a.ws, 0, 1);
  stage_x(rx, lds0, 2, off, w, vx);
  {
    const int head = (w >> 1) & 1, isk = w >> 2;
    const int kstep = (w >> 1) + (w & 1);
    const int cbase = isk * 40 + head * 20;
    const int fp = r;
    const int hh = (fp >> 2) & 1, ii = (fp & 3) + 4 * (fp >> 3);
    const bool fok = ii < 10;
    const int f = 10 * hh + ii;
    const LAS uint16_t* wp = (const LAS uint16_t*)(sm + QK_RAW) + head * 400;
    unsigned short e[8];
#pragma unroll
    for (int x = 0; x < 8; ++x) {
      const int c = 16 * kstep + 8 * (x >> 2) + 4 * h + (x & 3);
      const int d = c - cbase;
      e[x] = (fok && d >= 0 && d < 20) ? wp[d * 20 + f] : (unsigned short)0;
    }
    u32x4 v = {e[0] | ((unsigned)e[1] << 16), e[2] | ((unsigned)e[3] << 16), e[4] | ((unsigned)e[5] << 16), e[6] | ((unsigned)e[7] << 16)};
    *(LAS u32x4*)(smw + QK_PHI + w * 1024 + lane * 16) = v;
  }
  rdX(0, 0, 0); rdW(0, 0, 0, 0); rdW(0, 0, 0, 1); rdW(0, 0, 0, 2); rdW(0, 0, 0, 3);
  SB();
#pragma unroll
  for (int t = 0; t < 10; ++t) {
    const int tb = t - 1;
    step(0, 0, t, 1, true, tb, t > 0 ? 2 : -1, t > 0 ? 3 : -1);
    step(1, 1, t, 2, true, tb, t > 0 ? 4 : -1, -1);
    step(2, 0, t, 3, true, tb, t > 0 ? 5 : -1, -1);
    if (t < 9) {
      if (t < 8) { WAIT_BAR(4); } else { WAIT_BAR(0); }
      SB();
      step(3, 1, t + 1, 0, true, t, 0, 1);
    } else step(3, 1, 0, 0, false, 0, -1, -1);
  }
  STAMP(a.ws, 0, 2);
  LAS float* csp = (LAS float*)(smw + QK_MISC);
  LAS float* csl = csp + 1024 + w * 128;
#pragma unroll
  for (int n = 0; n < 4; ++n) { const float c = swap_add(cs[n]); if (h == 0) csp[w * 128 + n * 32 + r] = c; }
  LDS_BAR();
  {
#pragma unroll
    for (int n = 0; n < 2; ++n) {
      const int o = n * 64 + lane;
      float c = 0.f;
#pragma unroll
      for (int k = 0; k < 8; ++k) c += csp[k * 128 + o];
      csl[o] = 0.5f * c;
    }
  }
  const float s1 = swap_add(sum), s2 = swap_add(sq);
  const float mean = s1 * (1.f / 640.f);
  const float rstd = rsqrtf(fmaxf(s2 * (1.f / 640.f) - mean * mean, 0.f) + 1e-5f);
  const float ra = rstd, rb = -mean * rstd;
  asm volatile("s_waitcnt lgkmcnt(0)" ::: "memory");
  bf16x8 X[8];
#pragma unroll
  for (int n = 0; n < 4; ++n) {
#pragma unroll
    for (int g = 0; g < 4; ++g) {
      const f32x4 c4 = *(LAS f32x4*)(csl + n * 32 + 8 * g + 4 * h);
#pragma unroll
      for (int e = 0; e < 4; ++e) acc[n][4 * g + e] = ra * acc[n][4 * g + e] + rb * c4[e];
    }
#pragma unroll
    for (int u = 0; u < 2; ++u) {
      const u32x4 v = {pk2(acc[n][8 * u], acc[n][8 * u + 1]), pk2(acc[n][8 * u + 2], acc[n][8 * u + 3]), pk2(acc[n][8 * u + 4], acc[n][8 * u + 5]), pk2(acc[n][8 * u + 6], acc[n][8 * u + 7])};
      X[2 * n + u] = __builtin_bit_cast(bf16x8, v);
    }
  }
  constexpr int IMG = 0, IROW = 240;
  constexpr int ONES = 61440;
  constexpr int KVT = 62464;
  const int myrow = w * 32 + r;
  f32x16 z16;
#pragma unroll
  for (int e = 0; e < 16; ++e) z16[e] = 0.f;
  auto phi_frag = [&](int id) { return ld_b128(sm + QK_PHI + id * 1024 + lane * 16); };
#pragma unroll
  for (int hd = 0; hd < 2; ++hd) {
    f32x16 pk = mfma32(phi_frag(4 + 2 * hd), X[2 + hd], z16);
    pk = mfma32(phi_frag(5 + 2 * hd), X[3 + hd], pk);
    float L[20];
    dual_softmax(pk, L);
#pragma unroll
    for (int g = 0; g < 5; ++g) { const u32x2 v = {pk2(L[4 * g], L[4 * g + 1]), pk2(L[4 * g + 2], L[4 * g + 3])}; *(LAS u32x2*)(smw + IMG + myrow * IROW + hd * 80 + h * 40 + g * 8) = v; }
  }
  {
    const u32x4 a5 = __builtin_bit_cast(u32x4, X[5]), a6 = __builtin_bit_cast(u32x4, X[6]), a7 = __builtin_bit_cast(u32x4, X[7]);
    LAS char* vb = smw + IMG + myrow * IROW + 160 + h * 8;
    *(LAS u32x2*)(vb + 0) = (u32x2){a5[0], a5[1]};
    *(LAS u32x2*)(vb + 16) = (u32x2){a5[2], a5[3]};
    *(LAS u32x2*)(vb + 32) = (u32x2){a6[0], a6[1]};
    *(LAS u32x2*)(vb + 48) = (u32x2){a6[2], a6[3]};
    *(LAS u32x2*)(vb + 64) = (u32x2){a7[0], a7[1]};
  }
  if (w == 0 && lane == 0) *(LAS u32x2*)(smw + ONES) = (u32x2){0x00003f80u, 0u};
  if (w == 1) { const int hd = lane >> 5; *(LAS u32x2*)(smw + KVT + (hd * 32 + r) * 96 + 40) = (u32x2){0u, 0u}; *(LAS u32x2*)(smw + KVT + (hd * 32 + r) * 96 + 88) = (u32x2){0u, 0u}; }
  LDS_BAR();
  STAMP(a.ws, 0, 3);
  char* kvp = a.ws + WS_KVP + (size_t)(j * 8 + p) * KVP_STRIDE;
  const rsrc_t rkv = mk_rsrc(a.ws + WS_KVP, 16 * 8 * KVP_STRIDE);
  if (w < 6) {
    const int hd = w / 3, mt = w - hd * 3;
    const int q = lane >> 4, i = lane & 15;
    f32x4 d0 = {0.f, 0.f, 0.f, 0.f}, d1 = {0.f, 0.f, 0.f, 0.f};
#pragma unroll
    for (int ks = 0; ks < 8; ++ks) {
      bf16x4 av[2], b0[2], b1[2];
#pragma unroll
      for (int u = 0; u < 2; ++u) {
        const int row = 32 * ks + 8 * q + 4 * u + (i >> 2);
        const LAS char* rp = sm + IMG + row * IROW;
        av[u] = ld_tr(rp + hd * 80 + mt * 32 + (i & 3) * 8);
        b0[u] = ld_tr(rp + 160 + hd * 40 + (i & 3) * 8);
        const LAS char* p1 = (i & 3) == 0 ? rp + 160 + hd * 40 + 32 : sm + ONES;
        b1[u] = ld_tr(p1);
      }
      const bf16x8 A = cat(av[0], av[1]);
      d0 = mfma16(A, cat(b0[0], b0[1]), d0);
      d1 = mfma16(A, cat(b1[0], b1[1]), d1);
    }
    const unsigned base = (unsigned)((j * 8 + p) * KVP_STRIDE + hd * 3360 + mt * 1344);
    if (mt < 2 || q < 2) __builtin_amdgcn_raw_buffer_store_b128(__builtin_bit_cast(u32x4, d0), rkv, base + lane * 16, 0, 16);
    const unsigned o1 = base + (mt < 2 ? 1024 : 512);
    if (i < 5 && (mt < 2 || q < 2)) __builtin_amdgcn_raw_buffer_store_b128(__builtin_bit_cast(u32x4, d1), rkv, o1 + (q * 5 + i) * 16, 0, 16);
  }
  bf16x8 Fq[2][3];
#pragma unroll
  for (int hd = 0; hd < 2; ++hd) {
    f32x16 pq = mfma32(phi_frag(2 * hd), X[hd], z16);
    pq = mfma32(phi_frag(2 * hd + 1), X[hd + 1], pq);
    float L[20];
    dual_softmax(pq, L);
    const u32x4 f0 = {pk2(L[0], L[1]), pk2(L[2], L[3]), pk2(L[4], L[5]), pk2(L[6], L[7])};
    const u32x4 f1 = {pk2(L[8], L[9]), pk2(L[10], L[11]), pk2(L[12], L[13]), pk2(L[14], L[15])};
    const u32x4 f2 = {pk2(L[16], L[17]), pk2(L[18], L[19]), 0u, 0u};
    Fq[hd][0] = __builtin_bit_cast(bf16x8, f0); Fq[hd][1] = __builtin_bit_cast(bf16x8, f1); Fq[hd][2] = __builtin_bit_cast(bf16x8, f2);
  }
  GAS unsigned* flags = (GAS unsigned*)(a.ws + WS_KVFLAG);
  WAIT_BAR(0);
  STAMP(a.ws, 0, 4);
  if (w == 0) {
    if (lane == 0) __hip_atomic_store(flags + (j * 8 + p) * 16, tag, RLX_AGENT);
    for (unsigned spins = 0; spins < SPIN_CAP; ++spins) {
      const unsigned v = __hip_atomic_load(flags + (j * 8 + (lane & 7)) * 16, RLX_AGENT);
      if (__all(v == tag)) break;
      __builtin_amdgcn_s_sleep(1);
    }
  }
  asm volatile("s_barrier" ::: "memory");
  STAMP(a.ws, 0, 5);
  const int tid = w * 64 + lane;
  if (tid < 420) {
    const int hd = tid / 210, e = tid - hd * 210;
    f32x4 s = {0.f, 0.f, 0.f, 0.f};
    u32x4 v[8];
#pragma unroll
    for (int k = 0; k < 8; ++k) v[k] = __builtin_amdgcn_raw_buffer_load_b128(rkv, (unsigned)((j * 8 + k) * KVP_STRIDE + tid * 16), 0, 16);
#pragma unroll
    for (int k = 0; k < 8; ++k) s += __builtin_bit_cast(f32x4, v[k]);
    int mt, o;
    if (e < 84) { mt = 0; o = e; } else if (e < 168) { mt = 1; o = e - 84; } else { mt = 2; o = e - 168; }
    const int n0 = mt < 2 ? 64 : 32;
    int q, dc;
    if (o < n0) { q = o >> 4; dc = o & 15; } else { const int o2 = o - n0; q = o2 / 5; dc = 16 + (o2 - q * 5); }
    const int phi = 16 * mt + 4 * q;
    const int hh = phi >= 20 ? 1 : 0, m = phi - 20 * hh;
    const u32x2 val = {pk2(s[0], s[1]), pk2(s[2], s[3])};
    LAS char* kt = smw + KVT + hd * 32 * 96 + hh * 48 + m * 2;
    if (dc < 20) {
      int reg, half;
      if (hd == 0) { if (dc < 8) { reg = dc; half = 0; } else if (dc < 16) { reg = dc - 8; half = 1; } else { reg = dc - 8; half = 0; } }
      else { if (dc < 4) { reg = dc; half = 0; } else if (dc < 12) { reg = dc - 4; half = 1; } else { reg = dc - 8; half = 0; } }
      const int dp = (reg & 3) + 8 * (reg >> 2) + 4 * half;
      *(LAS u32x2*)(kt + dp * 96) = val;
    } else {
      *(LAS u32x2*)(kt + 24 * 96) = val; *(LAS u32x2*)(kt + 28 * 96) = val;
    }
  }
  LDS_BAR();
  STAMP(a.ws, 0, 6);
  float O[2][12];
#pragma unroll
  for (int hd = 0; hd < 2; ++hd) {
    f32x16 d = z16;
#pragma unroll
    for (int s = 0; s < 3; ++s) d = mfma32(ld_b128(sm + KVT + (hd * 32 + r) * 96 + h * 48 + s * 16), Fq[hd][s], d);
    const float inv = __builtin_amdgcn_rcpf(d[12] + 1e-6f);
#pragma unroll
    for (int e = 0; e < 12; ++e) O[hd][e] = d[e] * inv;
  }
  {
    const rsrc_t rafr = mk_rsrc(a.ws + WS_AFR, (unsigned)AFR_BYTES);
    const int row = p * 256 + myrow;
    const unsigned rowbase = (unsigned)(((row >> 4) * KS * 64 + (row & 15)) * 16);
    auto put = [&](int pi, u32x4 v) { const int s0 = 40 * j + 8 * pi; __builtin_amdgcn_raw_buffer_store_b128(v, rafr, rowbase + (unsigned)(((s0 >> 5) * 64 + ((s0 & 31) >> 3) * 16) * 16), 0, SC1); };
    const u32x4 first = {pk2(O[0][0], O[0][1]), pk2(O[0][2], O[0][3]), pk2(O[0][4], O[0][5]), pk2(O[0][6], O[0][7])};
    put(h, first);
    const u32x4 mid0 = {pk2(O[0][8], O[0][9]), pk2(O[0][10], O[0][11]), pk2(O[1][0], O[1][1]), pk2(O[1][2], O[1][3])};
    const u32x4 mid1 = {pk2(O[1][0], O[1][1]), pk2(O[1][2], O[1][3]), pk2(O[1][4], O[1][5]), pk2(O[1][6], O[1][7])};
    put(2 + h, h ? mid1 : mid0);
    if (h == 0) { const u32x4 last = {pk2(O[1][4], O[1][5]), pk2(O[1][6], O[1][7]), pk2(O[1][8], O[1][9]), pk2(O[1][10], O[1][11])}; put(4, last); }
  }
  STAMP(a.ws, 0, 7);
}

DI void gemm2_body(const K1Args& a, char* smem, int w, int lane, int b) {
  const int tid = w * 64 + lane;
  const int xcd = b & 7, idx = b >> 3;
  const int rt64 = xcd * 4 + (idx >> 3), ct80 = idx & 7;
  u32x4 xr[2];
#pragma unroll
  for (int i = 0; i < 2; ++i) {
    const int it = tid + 512 * i;
    if (it < 640) { const int row = it / 10, g = it - row * 10; xr[i] = *(const u32x4*)(a.x + (size_t)(rt64 * 64 + row) * 640 + ct80 * 80 + g * 8); }
  }
  LAS float* red = (LAS float*)smem;
  if (w < 4) {
    const rsrc_t ra = mk_rsrc(a.ws + WS_AFR + (size_t)rt64 * 4 * KS * 1024, 4 * KS * 1024);
    const rsrc_t rb = mk_rsrc(a.ws + WS_BFR + (size_t)ct80 * 5 * KS * 1024, 5 * KS * 1024);
    constexpr int DEPTH = 4, NS = 17;
    u32x4 A[DEPTH][4], B[DEPTH][5];
    const unsigned voff = lane * 16, sbase = w * NS * 1024;
    const int off = (ct80 * 2 + (idx >> 3) * 4) % NS;
    auto koff = [&](int s) { int k = s + off; if (k >= NS) k -= NS; return sbase + (unsigned)k * 1024u; };
    auto ldB = [&](int s, int c) { B[s % DEPTH][c] = __builtin_amdgcn_raw_buffer_load_b128(rb, voff, koff(s) + c * KS * 1024, SC1); };
    auto ldA = [&](int s, int r) { A[s % DEPTH][r] = __builtin_amdgcn_raw_buffer_load_b128(ra, voff, koff(s) + r * KS * 1024, SC1); };
#pragma unroll
    for (int s = 0; s < DEPTH; ++s) {
#pragma unroll
      for (int c = 0; c < 5; ++c) ldB(s, c);
      if (s < DEPTH - 1) {
#pragma unroll
        for (int r = 0; r < 4; ++r) ldA(s, r);
      }
    }
    f32x4 acc[5][4];
#pragma unroll
    for (int c = 0; c < 5; ++c)
#pragma unroll
      for (int r = 0; r < 4; ++r) acc[c][r] = (f32x4){0.f, 0.f, 0.f, 0.f};
    SB();
#pragma unroll
    for (int s = 0; s < NS; ++s) {
      const int st = s % DEPTH;
#pragma unroll
      for (int c = 0; c < 5; ++c) {
#pragma unroll
        for (int r = 0; r < 4; ++r) acc[c][r] = mfma16(__builtin_bit_cast(bf16x8, B[st][c]), __builtin_bit_cast(bf16x8, A[st][r]), acc[c][r]);
        SB();
        if (c < 4 && s - 1 + DEPTH < NS) ldA(s - 1 + DEPTH, c);
        if (s + DEPTH < NS) ldB(s + DEPTH, c);
        SB();
      }
    }
    STAMP(a.ws, 1, 2);
#pragma unroll
    for (int c = 0; c < 5; ++c)
#pragma unroll
      for (int r = 0; r < 4; ++r) *(LAS f32x4*)(red + (w * 64 + r * 16 + (lane & 15)) * 84 + c * 16 + (lane >> 4) * 4) = acc[c][r];
  }
  LDS_BAR();
#pragma unroll
  for (int i = 0; i < 2; ++i) {
    const int it = tid + 512 * i;
    if (it < 640) {
      const int row = it / 10, g = it - row * 10;
      f32x4 s0 = {0.f, 0.f, 0.f, 0.f}, s1 = {0.f, 0.f, 0.f, 0.f};
#pragma unroll
      for (int k = 0; k < 4; ++k) { s0 += *(LAS f32x4*)(red + (k * 64 + row) * 84 + g * 8); s1 += *(LAS f32x4*)(red + (k * 64 + row) * 84 + g * 8 + 4); }
      u32x4 o;
#pragma unroll
      for (int e = 0; e < 4; ++e) {
        const float lo = __uint_as_float(xr[i][e] << 16), hi = __uint_as_float(xr[i][e] & 0xffff0000u);
        const float v0 = e < 2 ? s0[2 * e] : s1[2 * e - 4], v1 = e < 2 ? s0[2 * e + 1] : s1[2 * e - 3];
        o[e] = pk2(lo + v0, hi + v1);
      }
      *(u32x4*)(a.out + (size_t)(rt64 * 64 + row) * 640 + ct80 * 80 + g * 8) = o;
    }
  }
}

__global__ __launch_bounds__(512, 2) void fused_kernel(K1Args a) {
  extern __shared__ __attribute__((aligned(16))) char smem[];
  const int tid = threadIdx.x, lane = tid & 63, w = rfl(tid >> 6);
  const int b = blockIdx.x, xcd = b & 7, idx = b >> 3;
  const int p = idx & 7, jj = (idx >> 3) & 1, j = xcd * 2 + jj;
  STAMP(a.ws, 0, 0);
  const unsigned tag = __hip_atomic_load((GAS unsigned*)(a.ws + WS_EPOCH), RLX_AGENT) + 1u;
  const int kind = (idx >> 4) * 2 + jj;
#define ROT 1
  const int off = ROT == 1 ? (p + 3 * kind) % 10 : ROT == 2 ? ((p + kind) & 1) : ROT == 3 ? ((p + kind) & 3) : 0;
  if (idx < 16) qkv_body(a, smem, p, j, w, lane, b, off, tag);
  else glu_body(a, smem, p, j, w, lane, b, off);
  WAIT_BAR(0);
  GAS unsigned* done = (GAS unsigned*)(a.ws + WS_DONE);
  if (w == 0) {
    if (lane == 0) __hip_atomic_store(done + b * 4, tag, RLX_AGENT);
    for (unsigned spins = 0; spins < SPIN_CAP; ++spins) {
      bool ok = true;
#pragma unroll
      for (int i = 0; i < 4; ++i) ok &= __hip_atomic_load(done + (i * 64 + lane) * 4, RLX_AGENT) == tag;
      if (__all(ok)) break;
      __builtin_amdgcn_s_sleep(1);
    }
    if (b == 0 && lane == 0) __hip_atomic_store((GAS unsigned*)(a.ws + WS_EPOCH), tag, RLX_AGENT);
  }
  asm volatile("s_barrier" ::: "memory");
  STAMP(a.ws, 1, 0);
  gemm2_body(a, smem, w, lane, b);
  STAMP(a.ws, 1, 7);
}

extern "C" void kernel_launch(void* const* d_in, const int* in_sizes, int n_in, void* d_out, int out_size, void* d_ws, size_t ws_size, hipStream_t stream) {
  (void)in_sizes; (void)n_in; (void)out_size; (void)ws_size;
  K1Args a;
  a.x = (const uint16_t*)d_in[0]; a.wqkv = (const uint16_t*)d_in[1]; a.wphi = (const uint16_t*)d_in[2]; a.wo = (const uint16_t*)d_in[3];
  a.wg = (const uint16_t*)d_in[5]; a.wu = (const uint16_t*)d_in[6]; a.wd = (const uint16_t*)d_in[7]; a.ws = (char*)d_ws; a.out = (uint16_t*)d_out;
  hipFuncSetAttribute((const void*)fused_kernel, hipFuncAttributeMaxDynamicSharedMemorySize, K1_LDS);
  fused_kernel<<<256, 512, K1_LDS, stream>>>(a);
}
```

```cpp
#include <hip/hip_runtime.h>
#include <stdint.h>

typedef __bf16 bf16x2 __attribute__((ext_vector_type(2)));
typedef __bf16 bf16x4 __attribute__((ext_vector_type(4)));
typedef __bf16 bf16x8 __attribute__((ext_vector_type(8)));
typedef float f32x2 __attribute__((ext_vector_type(2)));
typedef float f32x4 __attribute__((ext_vector_type(4)));
typedef unsigned u32x2 __attribute__((ext_vector_type(2)));
typedef unsigned u32x4 __attribute__((ext_vector_type(4)));
#define LAS __attribute__((address_space(3)))
#define GAS __attribute__((address_space(1)))
#define DI __device__ __forceinline__
typedef __amdgpu_buffer_rsrc_t rsrc_t;
#define RLX_AGENT __ATOMIC_RELAXED, __HIP_MEMORY_SCOPE_AGENT

constexpr int KS = 68;
constexpr size_t WS_EPOCH = 0;
constexpr size_t WS_KVFLAG = 4096;
constexpr size_t WS_DONE = 20480;
constexpr size_t WS_STAT = 32768;
constexpr size_t WS_CS = 65536;
constexpr int CS_TILE = 192 * 8;
constexpr size_t WS_KVP = 1u << 20;
constexpr int KVP_STRIDE = 8192;
constexpr size_t WS_AFR = 2u << 20;
constexpr size_t AFR_BYTES = (size_t)128 * KS * 1024;
constexpr size_t WS_BFR = WS_AFR + AFR_BYTES;
constexpr size_t BFR_BYTES = (size_t)40 * KS * 1024;

constexpr float LOG2E = 1.4426950408889634f;
constexpr int SC1 = 16;
constexpr unsigned OOB = 0x7ffffff0u;
constexpr unsigned SPIN_CAP = 1u << 18;

DI rsrc_t mk_rsrc(const void* p, unsigned bytes) { return __builtin_amdgcn_make_buffer_rsrc((void*)p, (short)0, (int)bytes, 0x00020000); }
DI unsigned rfl(unsigned v) { return __builtin_amdgcn_readfirstlane(v); }
DI void dma16(unsigned lds_addr, unsigned voff, rsrc_t r, unsigned soff) {
  asm volatile("s_mov_b32 m0, %0\n\ts_nop 0\n\tbuffer_load_dwordx4 %1, %2, %3 offen lds" ::"s"(lds_addr), "v"(voff), "s"(r), "s"(soff) : "m0", "memory");
}
DI void set_m0(unsigned lds_addr) { asm volatile("s_mov_b32 m0, %0" ::"s"(lds_addr) : "m0", "memory"); }
template <int OFF>
DI void dma16_off(unsigned voff, rsrc_t r, unsigned soff) {
  asm volatile("buffer_load_dwordx4 %0, %1, %2 offen offset:%3 lds" ::"v"(voff), "s"(r), "s"(soff), "i"(OFF) : "memory");
}
DI void dma16_i(int i, unsigned voff, rsrc_t r, unsigned soff) {
  if (i == 0) dma16_off<0>(voff, r, soff); else if (i == 1) dma16_off<1024>(voff, r, soff); else if (i == 2) dma16_off<2048>(voff, r, soff); else dma16_off<3072>(voff, r, soff);
}
DI void dma4_sc1(unsigned lds_addr, unsigned voff, rsrc_t r) {
  asm volatile("s_mov_b32 m0, %0\n\ts_nop 0\n\tbuffer_load_dword %1, %2, 0 offen sc1 lds" ::"s"(lds_addr), "v"(voff), "s"(r) : "m0", "memory");
}
DI void touch_line(unsigned lds_addr, unsigned voff, rsrc_t r, unsigned soff) {
  asm volatile("s_mov_b32 m0, %0\n\ts_nop 0\n\tbuffer_load_dword %1, %2, %3 offen lds" ::"s"(lds_addr), "v"(voff), "s"(r), "s"(soff) : "m0", "memory");
}
#define WAIT_BAR(N) asm volatile("s_waitcnt vmcnt(" #N ") lgkmcnt(0)\n\ts_barrier" ::: "memory")
#define WAIT_VM(N) asm volatile("s_waitcnt vmcnt(" #N ")" ::: "memory")
#define LDS_BAR() asm volatile("s_waitcnt lgkmcnt(0)\n\ts_barrier" ::: "memory")
#define LDS_WAIT() asm volatile("s_waitcnt lgkmcnt(0)" ::: "memory")
#define SB() __builtin_amdgcn_sched_barrier(0)
#define PIN1(x) asm volatile("" : "+v"(x))

DI bf16x8 ld_b128(const LAS char* p) { return *(const LAS bf16x8*)p; }
DI bf16x4 ld_tr(const LAS char* p) { return __builtin_amdgcn_ds_read_tr16_b64_v4bf16((LAS bf16x4*)p); }
DI bf16x8 cat(bf16x4 a, bf16x4 b) { return __builtin_shufflevector(a, b, 0, 1, 2, 3, 4, 5, 6, 7); }
DI f32x4 mfma16(bf16x8 a, bf16x8 b, f32x4 c) { return __builtin_amdgcn_mfma_f32_16x16x32_bf16(a, b, c, 0, 0, 0); }
DI float dot_sum(bf16x8 v, float acc) {
  const u32x4 w = __builtin_bit_cast(u32x4, v);
  const bf16x2 one = __builtin_bit_cast(bf16x2, 0x3f803f80u);
#pragma unroll
  for (int i = 0; i < 4; ++i) { const unsigned e = w[i]; acc = __builtin_amdgcn_fdot2_f32_bf16(__builtin_bit_cast(bf16x2, e), one, acc, false); }
  return acc;
}
DI float dot_sq(bf16x8 v, float acc) {
  const u32x4 w = __builtin_bit_cast(u32x4, v);
#pragma unroll
  for (int i = 0; i < 4; ++i) { const unsigned e = w[i]; const bf16x2 p = __builtin_bit_cast(bf16x2, e); acc = __builtin_amdgcn_fdot2_f32_bf16(p, p, acc, false); }
  return acc;
}
DI unsigned pk2(float a, float b) { bf16x2 p; p[0] = (__bf16)a; p[1] = (__bf16)b; return __builtin_bit_cast(unsigned, p); }
DI unsigned fu(float x) { return __float_as_uint(x); }
DI float uf(unsigned x) { return __uint_as_float(x); }
DI float q_add(float v) { auto a = __builtin_amdgcn_permlane16_swap(fu(v), fu(v), false, false); v = uf(a[0]) + uf(a[1]); auto b = __builtin_amdgcn_permlane32_swap(fu(v), fu(v), false, false); return uf(b[0]) + uf(b[1]); }
DI float mx2(float a, float b) { return __builtin_amdgcn_fmed3f(a, b, __builtin_inff()); }
DI float mn2(float a, float b) { return __builtin_amdgcn_fmed3f(a, b, -__builtin_inff()); }
DI float q_max(float v) { auto a = __builtin_amdgcn_permlane16_swap(fu(v), fu(v), false, false); v = mx2(uf(a[0]), uf(a[1])); auto b = __builtin_amdgcn_permlane32_swap(fu(v), fu(v), false, false); return mx2(uf(b[0]), uf(b[1])); }
DI float q_min(float v) { auto a = __builtin_amdgcn_permlane16_swap(fu(v), fu(v), false, false); v = mn2(uf(a[0]), uf(a[1])); auto b = __builtin_amdgcn_permlane32_swap(fu(v), fu(v), false, false); return mn2(uf(b[0]), uf(b[1])); }
DI float row_add(float v) {
  v += uf(__builtin_amdgcn_update_dpp(0u, fu(v), 0x128, 0xf, 0xf, false));
  v += uf(__builtin_amdgcn_update_dpp(0u, fu(v), 0x124, 0xf, 0xf, false));
  v += uf(__builtin_amdgcn_update_dpp(0u, fu(v), 0x122, 0xf, 0xf, false));
  v += uf(__builtin_amdgcn_update_dpp(0u, fu(v), 0x121, 0xf, 0xf, false));
  return v;
}
DI float bcast_q1(float v) { auto a = __builtin_amdgcn_permlane16_swap(fu(v), fu(v), false, false); auto b = __builtin_amdgcn_permlane32_swap(a[1], a[1], false, false); return uf(b[0]); }

constexpr int XS_SLOT = 32768;
constexpr int WS_OFF = 3 * XS_SLOT;
constexpr int WG_SLOT = 24576, WG_U = 12288;
constexpr int WQ_SLOT = 16384;
constexpr int GL_MISC = WS_OFF + 2 * WG_SLOT;
constexpr int QK_PHI = WS_OFF + 2 * WQ_SLOT;
constexpr int QK_RAW = QK_PHI + 8192;
constexpr int QK_MISC = QK_RAW + 2048;
constexpr int SCR_ROW = 150528;
constexpr int SCR_TAG = SCR_ROW + 10240;
constexpr int SCR_DUMMY = SCR_TAG + 2048;
constexpr int SCR_SINK = 148480;
constexpr int G_CS = 150528, G_ST = G_CS + 9216;
constexpr int LDS_BYTES = 160 * 1024;

#define STAMP(ws, kern, id) do {} while (0)

#define PSTAMP(arr, i) do {} while (0)
#define PDUMP(ws, arr, n) do {} while (0)
#define PSTAMP2(arr, i) do {} while (0)
#define PDUMP2(ws, arr, n) do {} while (0)
struct Args { const uint16_t *x, *wqkv, *wphi, *wo, *wg, *wu, *wd; char* ws; uint16_t* out; };

DI int rot10(int t, int off) { const int v = t + off; return v >= 10 ? v - 10 : v; }

DI void relayout_issue(const Args& a, int unit, unsigned scratch, int lane) {
  const int ks = unit / 10, cg = unit - ks * 10;
  const rsrc_t r = ks < 20 ? mk_rsrc(a.wo, 640 * 640 * 2) : mk_rsrc(a.wd, 1536 * 640 * 2);
  const unsigned row0 = ks < 20 ? ks * 32 : (ks - 20) * 32;
  const unsigned voff = (lane >> 3) * 1280 + (lane & 7) * 16;
#pragma unroll
  for (int i = 0; i < 4; ++i) dma16(scratch + i * 1024, voff, r, (row0 + 8 * i) * 1280 + cg * 128);
}
DI void relayout_touch(const Args& a, int unit, unsigned dummy, int lane) {
  const int ks = unit / 10, cg = unit - ks * 10;
  const rsrc_t r = ks < 20 ? mk_rsrc(a.wo, 640 * 640 * 2) : mk_rsrc(a.wd, 1536 * 640 * 2);
  const unsigned row0 = ks < 20 ? ks * 32 : (ks - 20) * 32;
  touch_line(dummy, (lane < 32 && unit < 680) ? (row0 + lane) * 1280 + cg * 128 : OOB, r, 0);
}
DI void relayout_finish(const Args& a, int unit, const LAS char* sc, int lane) {
  const int ks = unit / 10, cg = unit - ks * 10;
  const int q = lane >> 4, i = lane & 15;
  const rsrc_t rb = mk_rsrc(a.ws + WS_BFR, (unsigned)BFR_BYTES);
  const unsigned dst = ((cg * 4) * KS + ks) * 1024 + lane * 16;
#pragma unroll
  for (int c4 = 0; c4 < 4; ++c4) {
    bf16x4 v[2];
#pragma unroll
    for (int u = 0; u < 2; ++u) {
      const int rb0 = ks < 20 ? 8 * q + 4 * u : 16 * u + 4 * q;
      v[u] = ld_tr(sc + (rb0 + (i >> 2)) * 128 + c4 * 32 + (i & 3) * 8);
    }
    __builtin_amdgcn_raw_buffer_store_b128(__builtin_bit_cast(u32x4, cat(v[0], v[1])), rb, dst, c4 * KS * 1024, SC1);
  }
}
DI void rowstat_issue(const Args& a, int row, unsigned scratch, int lane) {
  const rsrc_t r = mk_rsrc(a.x + (size_t)row * 640, 1280);
  dma16(scratch, lane * 16, r, 0);
  if (lane < 16) dma16(scratch + 1024, lane * 16, r, 1024);
}
struct RowStat { bf16x8 v0, v1; float s, q; };
DI void rowstat_a(RowStat& r, const LAS char* sc, int lane) { r.v0 = ld_b128(sc + lane * 16); r.v1 = ld_b128(sc + 1024 + (lane & 15) * 16); }
DI void rowstat_b(RowStat& r, int lane) {
  float s = dot_sum(r.v0, 0.f), q = dot_sq(r.v0, 0.f);
  const float s1 = dot_sum(r.v1, 0.f), q1 = dot_sq(r.v1, 0.f);
  r.s = s + (lane < 16 ? s1 : 0.f); r.q = q + (lane < 16 ? q1 : 0.f);
  asm volatile("" : "+v"(r.s), "+v"(r.q));
}
DI void rowstat_c(RowStat& r) { r.s = row_add(r.s); r.q = row_add(r.q); asm volatile("" : "+v"(r.s), "+v"(r.q)); }
DI void rowstat_d(const Args& a, RowStat& r, int row, int lane, unsigned tag) {
  const float s = q_add(r.s), q = q_add(r.q);
  const float mean = s * (1.f / 640.f);
  const float rstd = rsqrtf(fmaxf(q * (1.f / 640.f) - mean * mean, 0.f) + 1e-5f);
  const rsrc_t rs = mk_rsrc(a.ws + WS_STAT, 2048 * 16);
  const u32x4 g = {fu(mean), tag, fu(rstd), tag};
  __builtin_amdgcn_raw_buffer_store_b128(g, rs, lane == 0 ? (unsigned)(row * 16) : OOB, 0, SC1);
}

DI void x_offsets(int w, int lane, unsigned (&vx)[4]) {
#pragma unroll
  for (int i = 0; i < 4; ++i) vx[i] = (w * 32 + 8 * i + (lane >> 3)) * 1280 + (((lane & 7) ^ ((lane >> 4) | (4 * (i & 1)))) * 16) - i * 1024;
}
DI void x_m0(unsigned lds0, int t, int w) { set_m0(lds0 + (t % 3) * XS_SLOT + w * 4096); }
DI void x_tile(const rsrc_t rx, unsigned lds0, int t, int off, int w, const unsigned (&vx)[4]) {
  x_m0(lds0, t, w);
  asm volatile("s_nop 0");
  const unsigned so = rot10(t, off) * 128;
#pragma unroll
  for (int i = 0; i < 4; ++i) dma16_i(i, vx[i], rx, so);
}

DI void dma16_sc1(unsigned lds_addr, unsigned voff, rsrc_t r, unsigned soff) {
  asm volatile("s_mov_b32 m0, %0\n\ts_nop 0\n\tbuffer_load_dwordx4 %1, %2, %3 offen sc1 lds" ::"s"(lds_addr), "v"(voff), "s"(r), "s"(soff) : "m0", "memory");
}
template <int NCOL>
DI void gather_piece(const Args& a, int kind, int j, int p, unsigned lds0, int lane, int i) {
  constexpr int NC = NCOL * 6 * 8 / 1024;
  const bool isc = i < NC;
  const char* base = isc ? a.ws + WS_CS + (size_t)(kind * 16 + j) * 6 * CS_TILE : a.ws + WS_STAT + (size_t)p * 256 * 16;
  const rsrc_t r = mk_rsrc(base, isc ? 6 * CS_TILE : (i < NC + 4 ? 256 * 16 : 0));
  const unsigned dst = isc ? G_CS + i * 1024 : (i < NC + 4 ? G_ST + (i - NC) * 1024 : SCR_SINK);
  const unsigned so = isc ? (NCOL == 192 ? i * 1024 : i * CS_TILE) : (i - NC) * 1024;
  dma16_sc1(lds0 + dst, lane * 16, r, so);
}
template <int NCOL>
DI void gather_finish(const Args& a, int kind, int j, int p, int tid, const LAS char* sm, unsigned tag, LAS float* cs, LAS float* st) {
  if (tid < NCOL) {
    u32x2 g[6];
#pragma unroll
    for (int t = 0; t < 6; ++t) g[t] = *(const LAS u32x2*)(sm + G_CS + (t * NCOL + tid) * 8);
    for (unsigned spins = 0; spins < SPIN_CAP; ++spins) {
      bool ok = true;
#pragma unroll
      for (int t = 0; t < 6; ++t) ok &= g[t][1] == tag;
      if (__all(ok)) break;
      __builtin_amdgcn_s_sleep(2);
      const rsrc_t rc = mk_rsrc(a.ws + WS_CS + (size_t)(kind * 16 + j) * 6 * CS_TILE, 6 * CS_TILE);
#pragma unroll
      for (int t = 0; t < 6; ++t) g[t] = __builtin_amdgcn_raw_buffer_load_b64(rc, tid * 8, t * CS_TILE, SC1);
    }
    cs[tid] = ((uf(g[0][0]) + uf(g[1][0])) + (uf(g[2][0]) + uf(g[3][0]))) + (uf(g[4][0]) + uf(g[5][0]));
  }
  if (tid >= 256) {
    u32x4 g = *(const LAS u32x4*)(sm + G_ST + (tid - 256) * 16);
    for (unsigned spins = 0; spins < SPIN_CAP; ++spins) {
      if (__all((g[1] == tag) & (g[3] == tag))) break;
      __builtin_amdgcn_s_sleep(2);
      const rsrc_t rs = mk_rsrc(a.ws + WS_STAT + (size_t)p * 256 * 16, 256 * 16);
      g = __builtin_amdgcn_raw_buffer_load_b128(rs, (tid - 256) * 16, 0, SC1);
    }
    const float rstd = uf(g[2]); const f32x2 v = {rstd, -uf(g[0]) * rstd};
    *(LAS f32x2*)(st + (tid - 256) * 2) = v;
  }
}
DI bool cs_owner(int p, int t) { return t < 5 && (p == t || p == 7); }
DI bool cs_last(int p, int t) { return p == 7 ? t == 4 : t == p; }
DI void cs_publish(const Args& a, int kind, int j, int p, int col0, float s, int lane, unsigned tag) {
  s = q_add(s);
  const rsrc_t rc = mk_rsrc(a.ws + WS_CS + (size_t)(kind * 16 + j) * 6 * CS_TILE, 6 * CS_TILE);
  const u32x2 g = {fu(s), tag};
  const bool on = lane < 16 && col0 >= 0 && (p < 5 || p == 7);
  __builtin_amdgcn_raw_buffer_store_b64(g, rc, on ? (unsigned)((col0 + lane) * 8 + (p == 7 ? 5 : p) * CS_TILE) : OOB, 0, SC1);
}
template <int N>
DI void flags_load(const Args& a, int first, int lane, unsigned (&v)[N]) {
  GAS unsigned* f = (GAS unsigned*)(a.ws + WS_DONE);
#pragma unroll
  for (int i = 0; i < N; ++i) v[i] = __hip_atomic_load(f + (first + i * 64 + lane) * 4, RLX_AGENT);
}
template <int N>
DI void flags_wait(const Args& a, int first, int lane, unsigned (&v)[N], unsigned tag) {
  for (unsigned spins = 0; spins < SPIN_CAP; ++spins) {
    bool ok = true;
#pragma unroll
    for (int i = 0; i < N; ++i) ok &= v[i] == tag;
    if (__all(ok)) break;
    __builtin_amdgcn_s_sleep(1);
    flags_load<N>(a, first, lane, v);
  }
}

DI void g2_preload_piece(const Args& a, unsigned lds0, int b, int lane, int f);
DI void glu_body(const Args& a, char* smem, int p, int j, int w, int lane, int b, int off, unsigned& tag) {
  const LAS char* sm = (const LAS char*)smem;
  const unsigned lds0 = (unsigned)(uintptr_t)sm;
  const int r16 = lane & 15, q = lane >> 4, tid = w * 64 + lane;
  const int rq = w >> 1, ch = w & 1;
  const rsrc_t rx = mk_rsrc(a.x + (size_t)p * 256 * 640, 256 * 1280);
  const rsrc_t rw = mk_rsrc((w < 4 ? a.wg : a.wu), 640 * 3072);
  unsigned vx[4];
  x_offsets(w, lane, vx);
  unsigned vw[3];
#pragma unroll
  for (int i = 0; i < 3; ++i) {
    const int P = ((w & 3) * 3 + i) * 64 + lane, row = P / 12, pos = P - row * 12;
    int pc = pos - 2 * ((row >> 3) & 1); if (pc < 0) pc += 12;
    vw[i] = row * 3072 + j * 192 + pc * 16 - i * 1024;
  }
  const unsigned wdst = lds0 + WS_OFF + (w < 4 ? 0 : WG_U) + (w & 3) * 3072;
  auto w_m0 = [&](int t) { set_m0(wdst + (t & 1) * WG_SLOT); };
  auto w_so = [&](int t) { return (unsigned)(rot10(t, off) * (64 * 3072)); };
#pragma unroll
  for (int t = 0; t < 2; ++t) {
    w_m0(t);
    const unsigned so = w_so(t);
    asm volatile("s_nop 0");
#pragma unroll
    for (int i = 0; i < 3; ++i) dma16_i(i, vw[i], rw, so);
    x_tile(rx, lds0, t, off, w, vx);
  }
  x_tile(rx, lds0, 2, off, w, vx);
  {
    const unsigned row = 80 * p + (w & 3) * 20 + (lane >> 1);
    if (lane < 40) touch_line(lds0 + SCR_DUMMY, row * 3072 + ((j * 192) & ~127) + (lane & 1) * 128, rw, 0);
  }
  const int unit = (((b >> 3) - 16) * 8 + (b & 7)) * 8 + w;
  const bool job = unit < 680;
  const int row0 = (((b >> 3) - 16) * 8 + (b & 7)) * 16 + w * 2;
  rowstat_issue(a, row0, lds0 + SCR_ROW + w * 1280, lane);
  unsigned xa[2], wa[3], ca[2];
#pragma unroll
  for (int s = 0; s < 2; ++s) xa[s] = (rq * 64 + r16) * 128 + (((4 * s + q) ^ (r16 >> 1)) * 16);
#pragma unroll
  for (int c = 0; c < 3; ++c) { int pos = 3 * ch + c + (q & 1); if (pos >= 6) pos -= 6; wa[c] = WS_OFF + (8 * q + (r16 >> 2)) * 192 + pos * 32 + (r16 & 3) * 8; }
#pragma unroll
  for (int i = 0; i < 2; ++i) {
    const int g = (rq + 4 * i) % 6, c = g >> 1, im = g & 1; int pos = 3 * ch + c + (q & 1); if (pos >= 6) pos -= 6;
    ca[i] = WS_OFF + im * WG_U + (8 * q + (r16 >> 2)) * 192 + pos * 32 + (r16 & 3) * 8;
  }
  f32x4 acc[6][4];
#pragma unroll
  for (int g = 0; g < 6; ++g)
#pragma unroll
    for (int rt = 0; rt < 4; ++rt) acc[g][rt] = (f32x4){0.f, 0.f, 0.f, 0.f};
  float cs[2] = {0.f, 0.f};
  bf16x8 cf[4]; RowStat rs;
  bf16x8 XF[2][4], WF[4];
  auto rdX = [&](int t, int s, int rt) { XF[s][rt] = ld_b128(sm + xa[s] + (t % 3) * XS_SLOT + rt * 2048); };
  auto rdW = [&](int G) {
    const int t = G / 12, s = (G % 12) / 6, g = G % 6, c = g >> 1, im = g & 1;
    const LAS char* pp = sm + wa[c] + (t & 1) * WG_SLOT + im * WG_U + s * 6144;
    WF[G % 4] = cat(ld_tr(pp), ld_tr(pp + 768));
  };
  unsigned wso = 0, xso = 0;
  auto dma_pre = [&](int tb, int k) {
    if (k == 0 && tb + 2 < 10) { w_m0(tb + 2); wso = w_so(tb + 2); }
    if (k == 3 && tb + 3 < 10) { x_m0(lds0, tb + 3, w); xso = rot10(tb + 3, off) * 128; }
  };
  auto dma_piece = [&](int tb, int k) {
    if (k < 3) { if (tb + 2 < 10) dma16_i(k, vw[k], rw, wso); }
    else if (tb + 3 < 10) dma16_i(k - 3, vx[k - 3], rx, xso);
  };
  WAIT_BAR(14);
  if (w >= 4) __builtin_amdgcn_s_setprio(1);
  STAMP(a.ws, 0, 1);
  tag = *(const LAS unsigned*)(sm + SCR_TAG + w * 256) + 1u;
#pragma unroll
  for (int rt = 0; rt < 4; ++rt) rdX(0, 0, rt);
  rdW(0); rdW(1); rdW(2);
  SB();
  unsigned long long ps[20]; (void)ps;
  PSTAMP(ps, 0);
#pragma unroll
  for (int t = 0; t < 10; ++t) {
#pragma unroll
    for (int s = 0; s < 2; ++s) {
#pragma unroll
      for (int g = 0; g < 6; ++g) {
        const int G = 12 * t + 6 * s + g;
        if (s == 1 && g == 3 && t < 9) {
          PSTAMP(ps, 2 * t + 1);
          if (t == 0) { WAIT_BAR(7); }
          else if (t < 8) { WAIT_BAR(4); } else { WAIT_BAR(0); }
          if (t == 2) rowstat_issue(a, row0 + 1, lds0 + SCR_ROW + w * 1280, lane);
          if (t == 7 && job) relayout_issue(a, unit, lds0 + XS_SLOT + w * 4096, lane);
          PSTAMP(ps, 2 * t + 2);
          SB();
        }
        const bf16x8 Wc = WF[G % 4];
        acc[g][0] = mfma16(Wc, XF[s][0], acc[g][0]);
        acc[g][1] = mfma16(Wc, XF[s][1], acc[g][1]);
        if (G + 3 < 120) rdW(G + 3);
        if (s == 1 && g >= 3 && t < 9) dma_pre(t, g - 3);
        if (s == 0 && g <= 3 && t > 0) dma_pre(t - 1, g + 3);
        SB();
        acc[g][2] = mfma16(Wc, XF[s][2], acc[g][2]);
        acc[g][3] = mfma16(Wc, XF[s][3], acc[g][3]);
        if (s == 0 && g <= 3) rdX(t, 1, g);
        if (s == 1 && (g == 3 || g == 4) && t < 9) { rdX(t + 1, 0, 2 * (g - 3)); rdX(t + 1, 0, 2 * (g - 3) + 1); }
        if (s == 1 && g >= 3 && t < 9) dma_piece(t, g - 3);
        if (s == 0 && g <= 3 && t > 0) dma_piece(t - 1, g + 3);
        if (t == 2 || t == 4) {
          if (s == 0 && g == 0) rowstat_a(rs, sm + SCR_ROW + w * 1280, lane);
          if (s == 0 && g == 2) rowstat_b(rs, lane);
          if (s == 0 && g == 4) rowstat_c(rs);
          if (s == 1 && g == 0) rowstat_d(a, rs, row0 + (t == 4), lane, tag);
        }
        if (t < 5) {
          if (s == 0 && g == 1) {
#pragma unroll
            for (int i = 0; i < 2; ++i)
#pragma unroll
              for (int s2 = 0; s2 < 2; ++s2) { const LAS char* pp = sm + ca[i] + (t & 1) * WG_SLOT + s2 * 6144; cf[2 * i + s2] = cat(ld_tr(pp), ld_tr(pp + 768)); }
          }
          if (s == 0 && g == 4) { const float d = dot_sum(cf[1], dot_sum(cf[0], 0.f)); cs[0] += cs_owner(p, t) ? d : 0.f; PIN1(cs[0]); }
          if (s == 1 && g == 0) { const float d = dot_sum(cf[3], dot_sum(cf[2], 0.f)); cs[1] += cs_owner(p, t) ? d : 0.f; PIN1(cs[1]); }
        }
        if (t == 5 && s == 0 && g < 2) {
          const int g2 = rq + 4 * g, c = g2 >> 1, im = g2 & 1;
          cs_publish(a, 1, j, p, (g == 0 || rq < 2) ? im * 96 + (3 * ch + c) * 16 : -1, cs[g], lane, tag);
        }
        if (t == 8 && s == 0 && g < 2) gather_piece<192>(a, 1, j, p, lds0, lane, w + 8 * g);
        if (t == 1 && s == 0 && g == 4) relayout_touch(a, unit, lds0 + SCR_DUMMY, lane);
        if (t == 5 && s == 0 && g == 4) {
          const int l = (((b >> 3) - 16) * 8 + w) * 40 + lane, r = l / 10, c = l - r * 10, st = r >> 5;
          touch_line(lds0 + SCR_DUMMY, lane < 40 ? (unsigned)((((st >> 2) * 12 + (st & 3)) * 32 + (r & 31)) * 1280 + c * 128) : OOB, mk_rsrc(a.wd, 1536 * 1280), 0);
        }
        if (t == 9 && s == 0 && g == 1 && job) relayout_finish(a, unit, sm + XS_SLOT + w * 4096, lane);
        SB();
      }
    }
  }
  PSTAMP(ps, 19);
  PDUMP(a.ws, ps, 20);
  STAMP(a.ws, 0, 2);
  LAS float* csl = (LAS float*)(sm + GL_MISC);
  LAS float* stl = csl + 192;
  __builtin_amdgcn_s_setprio(0);
  gather_finish<192>(a, 1, j, p, tid, sm, tag, csl, stl);
  STAMP(a.ws, 0, 11);
  LDS_BAR();
  STAMP(a.ws, 0, 12);
  const rsrc_t rafr = mk_rsrc(a.ws + WS_AFR, (unsigned)AFR_BYTES);
  f32x4 cgv[3], cuv[3];
#pragma unroll
  for (int c = 0; c < 3; ++c) { cgv[c] = *(LAS f32x4*)(csl + (3 * ch + c) * 16 + 4 * q); cuv[c] = *(LAS f32x4*)(csl + 96 + (3 * ch + c) * 16 + 4 * q); }
  f32x2 st4[4];
#pragma unroll
  for (int rt = 0; rt < 4; ++rt) st4[rt] = *(LAS f32x2*)(stl + (rq * 64 + rt * 16 + r16) * 2);
  LDS_BAR();
#pragma unroll
  for (int i = 0; i < 10; ++i) g2_preload_piece(a, lds0, b, lane, w * 10 + i);
#pragma unroll
  for (int rt = 0; rt < 4; ++rt) {
    const f32x2 st = st4[rt];
    const float ga = -LOG2E * st[0], gb = -LOG2E * st[1];
    unsigned hp[3][2];
#pragma unroll
    for (int c = 0; c < 3; ++c) {
      float hv[4];
#pragma unroll
      for (int e = 0; e < 4; ++e) {
        const float ex = __builtin_amdgcn_exp2f(ga * acc[2 * c][rt][e] + gb * cgv[c][e]);
        const float u = st[0] * acc[2 * c + 1][rt][e] + st[1] * cuv[c][e];
        hv[e] = u * __builtin_amdgcn_rcpf(1.f + ex);
      }
      hp[c][0] = pk2(hv[0], hv[1]); hp[c][1] = pk2(hv[2], hv[3]);
    }
    const unsigned base = (unsigned)(((p * 16 + rq * 4 + rt) * KS + 20 + 3 * j) * 1024 + lane * 16);
    if (ch == 0) {
      __builtin_amdgcn_raw_buffer_store_b128((u32x4){hp[0][0], hp[0][1], hp[1][0], hp[1][1]}, rafr, base, 0, SC1);
      __builtin_amdgcn_raw_buffer_store_b64((u32x2){hp[2][0], hp[2][1]}, rafr, base + 1024, 0, SC1);
    } else {
      __builtin_amdgcn_raw_buffer_store_b64((u32x2){hp[0][0], hp[0][1]}, rafr, base + 1024 + 8, 0, SC1);
      __builtin_amdgcn_raw_buffer_store_b128((u32x4){hp[1][0], hp[1][1], hp[2][0], hp[2][1]}, rafr, base + 2048, 0, SC1);
    }
  }
  STAMP(a.ws, 0, 7);
}

DI void dual_softmax4(const f32x4 (&d1)[4], const f32x4 (&d2)[4], float (&L)[4][10]) {
  float mx[4], mn[4], s1[4], s2[4];
#pragma unroll
  for (int r = 0; r < 4; ++r) {
    mx[r] = mx2(mx2(mx2(d1[r][0], d1[r][1]), mx2(d1[r][2], d1[r][3])), d2[r][0]);
    mn[r] = mn2(mn2(mn2(d1[r][0], d1[r][1]), mn2(d1[r][2], d1[r][3])), d2[r][0]);
  }
#pragma unroll
  for (int r = 0; r < 4; ++r) { mx[r] = q_max(mx[r]); mn[r] = q_min(mn[r]); }
#pragma unroll
  for (int r = 0; r < 4; ++r) {
    const float a1 = -mx[r] * LOG2E, a2 = mn[r] * LOG2E;
    s1[r] = 0.f; s2[r] = 0.f;
#pragma unroll
    for (int i = 0; i < 5; ++i) {
      const float p = i < 4 ? d1[r][i & 3] : d2[r][0];
      L[r][i] = __builtin_amdgcn_exp2f(p * LOG2E + a1); s1[r] += L[r][i];
      L[r][5 + i] = __builtin_amdgcn_exp2f(a2 - p * LOG2E); s2[r] += L[r][5 + i];
    }
  }
#pragma unroll
  for (int r = 0; r < 4; ++r) { s1[r] = __builtin_amdgcn_rcpf(q_add(s1[r])); s2[r] = __builtin_amdgcn_rcpf(q_add(s2[r])); }
#pragma unroll
  for (int r = 0; r < 4; ++r)
#pragma unroll
    for (int i = 0; i < 5; ++i) { L[r][i] *= s1[r]; L[r][5 + i] *= s2[r]; }
}

DI void qkv_body(const Args& a, char* smem, int p, int j, int w, int lane, int b, int off, unsigned& tag) {
  const LAS char* sm = (const LAS char*)smem;
  LAS char* smw = (LAS char*)smem;
  const unsigned lds0 = (unsigned)(uintptr_t)sm;
  const int r16 = lane & 15, q = lane >> 4, tid = w * 64 + lane;
  const int rq = w >> 1, hd = w & 1;
  const rsrc_t rx = mk_rsrc(a.x + (size_t)p * 256 * 640, 256 * 1280);
  const rsrc_t rw = mk_rsrc(a.wqkv, 640 * 3840);
  unsigned vx[4];
  x_offsets(w, lane, vx);
  unsigned vw;
  { const int row = 8 * w + (lane >> 4), rot = (lane >> 4) + 4 * (w & 1); int pi = ((lane & 15) - 2 * rot) & 15; if (pi > 14) pi = 14; const int part = pi / 5, pc = pi - part * 5; vw = row * 3840 + part * 1280 + j * 80 + pc * 16; }
  const unsigned vw1 = vw + 4 * 3840 - 1024;
  auto w_m0 = [&](int t) { set_m0(lds0 + WS_OFF + (t & 1) * WQ_SLOT + 2 * w * 1024); };
  auto w_so = [&](int t) { return (unsigned)(rot10(t, off) * (64 * 3840)); };
#pragma unroll
  for (int t = 0; t < 2; ++t) {
    w_m0(t);
    const unsigned so = w_so(t);
    asm volatile("s_nop 0");
    dma16_off<0>(vw, rw, so); dma16_off<1024>(vw1, rw, so);
    x_tile(rx, lds0, t, off, w, vx);
  }
  x_tile(rx, lds0, 2, off, w, vx);
  {
    const int l6 = lane / 6, sub = lane - l6 * 6;
    const unsigned row = 80 * p + 10 * w + l6, o = (sub >> 1) * 1280 + j * 80 + (sub & 1) * 79;
    if (lane < 60) touch_line(lds0 + SCR_DUMMY, row * 3840 + (o & ~3u), rw, 0);
  }
  if (w == 7) {
    const rsrc_t rp = mk_rsrc(a.wphi + j * 800, 1600);
    dma16(lds0 + QK_RAW, lane * 16, rp, 0); dma16(lds0 + QK_RAW + 1024, lane * 16, rp, 1024);
  }
  unsigned xa[2], wa[4], ca;
#pragma unroll
  for (int s = 0; s < 2; ++s) xa[s] = (rq * 64 + r16) * 128 + (((4 * s + q) ^ (r16 >> 1)) * 16);
  {
    const int pp = r16 & 3, rot = (r16 >> 2) + 4 * (q & 1);
    const int g0 = 5 * hd + pp;
    const int g1 = pp == 0 ? 5 * hd + 4 : 10 + 5 * hd + pp - 1;
    const int g2 = pp < 2 ? 10 + 5 * hd + 3 + pp : 20 + 5 * hd + pp - 2;
    const int g3 = 20 + 5 * hd + 2 + (pp < 3 ? pp : 2);
    const int gg[4] = {g0, g1, g2, g3};
#pragma unroll
    for (int t = 0; t < 4; ++t) wa[t] = WS_OFF + (8 * q + (r16 >> 2)) * 256 + ((gg[t] + 4 * rot) & 31) * 8;
    const int gq = rq == 0 ? g0 : rq == 1 ? g1 : rq == 2 ? g2 : g3;
    ca = WS_OFF + (8 * q + (r16 >> 2)) * 256 + ((gq + 4 * rot) & 31) * 8;
  }
  f32x4 acc[4][4];
#pragma unroll
  for (int g = 0; g < 4; ++g)
#pragma unroll
    for (int rt = 0; rt < 4; ++rt) acc[g][rt] = (f32x4){0.f, 0.f, 0.f, 0.f};
  float cs = 0.f;
  bf16x8 cf[2]; unsigned pe[8];
  bf16x8 XF[2][4], WF[4];
  auto rdX = [&](int t, int s, int rt) { XF[s][rt] = ld_b128(sm + xa[s] + (t % 3) * XS_SLOT + rt * 2048); };
  auto rdW = [&](int G) {
    const int t = G / 8, s = (G % 8) / 4, g = G % 4;
    const LAS char* pp = sm + wa[g] + (t & 1) * WQ_SLOT + s * 8192;
    WF[G % 4] = cat(ld_tr(pp), ld_tr(pp + 1024));
  };
  unsigned wso = 0, xso = 0;
  auto dma_pre = [&](int tb, int k) {
    if (k == 0 && tb + 2 < 10) { w_m0(tb + 2); wso = w_so(tb + 2); }
    if (k == 2 && tb + 3 < 10) { x_m0(lds0, tb + 3, w); xso = rot10(tb + 3, off) * 128; }
  };
  auto dma_piece = [&](int tb, int k) {
    if (k == 0) { if (tb + 2 < 10) dma16_off<0>(vw, rw, wso); }
    else if (k == 1) { if (tb + 2 < 10) dma16_off<1024>(vw1, rw, wso); }
    else if (tb + 3 < 10) dma16_i(k - 2, vx[k - 2], rx, xso);
  };
  if (w == 7) { WAIT_BAR(13); } else { WAIT_BAR(11); }
  if (w >= 4) __builtin_amdgcn_s_setprio(1);
  STAMP(a.ws, 0, 1);
  tag = *(const LAS unsigned*)(sm + SCR_TAG + w * 256) + 1u;
#pragma unroll
  for (int rt = 0; rt < 4; ++rt) rdX(0, 0, rt);
  rdW(0); rdW(1); rdW(2);
  SB();
  unsigned long long ps[20]; (void)ps;
  PSTAMP(ps, 0);
#pragma unroll
  for (int t = 0; t < 10; ++t) {
#pragma unroll
    for (int s = 0; s < 2; ++s) {
#pragma unroll
      for (int g = 0; g < 4; ++g) {
        const int G = 8 * t + 4 * s + g;
        if (s == 1 && g == 1 && t < 9) {
          PSTAMP(ps, 2 * t + 1);
          if (t == 0) { if (w == 7) { WAIT_BAR(7); } else { WAIT_BAR(5); } }
          else if (t < 8) { WAIT_BAR(4); } else { WAIT_BAR(0); }
          PSTAMP(ps, 2 * t + 2);
          SB();
        }
        const bf16x8 Wc = WF[G % 4];
        acc[g][0] = mfma16(Wc, XF[s][0], acc[g][0]);
        acc[g][1] = mfma16(Wc, XF[s][1], acc[g][1]);
        if (G + 3 < 80) rdW(G + 3);
        if (s == 1 && g >= 1 && t < 9) dma_pre(t, g - 1);
        if (s == 0 && g <= 2 && t > 0) dma_pre(t - 1, g + 3);
        SB();
        acc[g][2] = mfma16(Wc, XF[s][2], acc[g][2]);
        acc[g][3] = mfma16(Wc, XF[s][3], acc[g][3]);
        if (s == 0 && g < 2) { rdX(t, 1, 2 * g); rdX(t, 1, 2 * g + 1); }
        if (s == 1 && (g == 1 || g == 2) && t < 9) { rdX(t + 1, 0, 2 * (g - 1)); rdX(t + 1, 0, 2 * (g - 1) + 1); }
        if (s == 1 && g >= 1 && t < 9) dma_piece(t, g - 1);
        if (s == 0 && g <= 2 && t > 0) dma_piece(t - 1, g + 3);
        if (t == 5) {
          if (s == 0 && g == 0) {
            const int head = w >> 2, isk = (w >> 1) & 1, ft = w & 1;
            const int qq = r16 >> 2, ee = r16 & 3;
            const bool fok = ft == 0 || ee == 0;
            const int f = 5 * qq + (ft == 0 ? ee : 4);
            const LAS uint16_t* wp = (const LAS uint16_t*)(sm + QK_RAW) + head * 400;
#pragma unroll
            for (int x = 0; x < 8; ++x) {
              int d;
              if (isk == 0) d = x < 4 ? 4 * q + x : (q == 0 ? 16 + x - 4 : -1);
              else d = x < 4 ? (q >= 1 ? 4 * (q - 1) + x : -1) : (q < 2 ? 12 + 4 * q + x - 4 : -1);
              const bool ok = fok && d >= 0;
              const unsigned v = wp[ok ? d * 20 + f : 0];
              pe[x] = ok ? v : 0u;
            }
          }
          if (s == 1 && g == 0) {
            const u32x4 v = {pe[0] | (pe[1] << 16), pe[2] | (pe[3] << 16), pe[4] | (pe[5] << 16), pe[6] | (pe[7] << 16)};
            *(LAS u32x4*)(smw + QK_PHI + w * 1024 + lane * 16) = v;
          }
        }
        if (t < 5) {
          if (s == 0 && g == 0) {
#pragma unroll
            for (int s2 = 0; s2 < 2; ++s2) { const LAS char* pp = sm + ca + (t & 1) * WQ_SLOT + s2 * 8192; cf[s2] = cat(ld_tr(pp), ld_tr(pp + 1024)); }
          }
          if (s == 0 && g == 3) { const float d = dot_sum(cf[1], dot_sum(cf[0], 0.f)); cs += cs_owner(p, t) ? d : 0.f; PIN1(cs); }
        }
        if (t == 6 && s == 0 && g == 0) cs_publish(a, 0, j, p, hd * 64 + rq * 16, cs, lane, tag);
        if (t == 8 && s == 0 && g < 2) gather_piece<128>(a, 0, j, p, lds0, lane, w + 8 * g);
        SB();
      }
    }
  }
  PSTAMP(ps, 19);
  PDUMP(a.ws, ps, 20);
  STAMP(a.ws, 0, 2);
  LAS float* csl = (LAS float*)(smw + QK_MISC);
  LAS float* stl = csl + 128;
  __builtin_amdgcn_s_setprio(0);
  gather_finish<128>(a, 0, j, p, tid, sm, tag, csl, stl);
  STAMP(a.ws, 0, 11);
  LDS_BAR();
  STAMP(a.ws, 0, 12);
  constexpr int IROW = 272, ONES = 256 * IROW, KVT = ONES + 64;
  f32x4 c4[4];
#pragma unroll
  for (int t = 0; t < 4; ++t) c4[t] = *(LAS f32x4*)(csl + hd * 64 + t * 16 + 4 * q);
  const f32x4 z4 = {0.f, 0.f, 0.f, 0.f};
  const bf16x8 Aq1 = ld_b128(sm + QK_PHI + (4 * hd + 0) * 1024 + lane * 16), Aq2 = ld_b128(sm + QK_PHI + (4 * hd + 1) * 1024 + lane * 16);
  const bf16x8 Ak1 = ld_b128(sm + QK_PHI + (4 * hd + 2) * 1024 + lane * 16), Ak2 = ld_b128(sm + QK_PHI + (4 * hd + 3) * 1024 + lane * 16);
  bf16x8 Xa[4];
  f32x2 st4[4];
#pragma unroll
  for (int rt = 0; rt < 4; ++rt) st4[rt] = *(LAS f32x2*)(stl + (rq * 64 + rt * 16 + r16) * 2);
  LDS_BAR();
#pragma unroll
  for (int i = 0; i < 10; ++i) g2_preload_piece(a, lds0, b, lane, w * 10 + i);
  unsigned pkd[4][4][2];
#pragma unroll
  for (int rt = 0; rt < 4; ++rt)
#pragma unroll
    for (int t = 0; t < 4; ++t) {
      const f32x4 v = st4[rt][0] * acc[t][rt] + st4[rt][1] * c4[t];
      pkd[rt][t][0] = pk2(v[0], v[1]); pkd[rt][t][1] = pk2(v[2], v[3]);
    }
  f32x4 dk1[4], dk2[4], dq1[4], dq2[4];
#pragma unroll
  for (int rt = 0; rt < 4; ++rt) {
    const bf16x8 Xb = __builtin_bit_cast(bf16x8, (u32x4){pkd[rt][1][0], pkd[rt][1][1], pkd[rt][2][0], pkd[rt][2][1]});
    dk1[rt] = mfma16(Ak1, Xb, z4); dk2[rt] = mfma16(Ak2, Xb, z4);
  }
#pragma unroll
  for (int rt = 0; rt < 4; ++rt) {
    Xa[rt] = __builtin_bit_cast(bf16x8, (u32x4){pkd[rt][0][0], pkd[rt][0][1], pkd[rt][1][0], pkd[rt][1][1]});
    dq1[rt] = mfma16(Aq1, Xa[rt], z4); dq2[rt] = mfma16(Aq2, Xa[rt], z4);
  }
  {
    float L[4][10];
    dual_softmax4(dk1, dk2, L);
#pragma unroll
    for (int rt = 0; rt < 4; ++rt) {
      LAS char* ir = smw + (rq * 64 + rt * 16 + r16) * IROW;
      LAS char* kp = ir + hd * 96 + q * 24;
      *(LAS u32x2*)(kp) = (u32x2){pk2(L[rt][0], L[rt][1]), pk2(L[rt][2], L[rt][3])};
      *(LAS u32x2*)(kp + 8) = (u32x2){pk2(L[rt][4], L[rt][5]), pk2(L[rt][6], L[rt][7])};
      *(LAS u32x2*)(kp + 16) = (u32x2){pk2(L[rt][8], L[rt][9]), 0u};
      LAS char* vp = ir + 192 + hd * 40;
      if (q >= 2) *(LAS u32x2*)(vp + (q - 2) * 8) = (u32x2){pkd[rt][2][0], pkd[rt][2][1]};
      if (q < 3) *(LAS u32x2*)(vp + (q + 2) * 8) = (u32x2){pkd[rt][3][0], pkd[rt][3][1]};
    }
  }
  STAMP(a.ws, 0, 13);
  if (tid == 0) *(LAS u32x2*)(smw + ONES) = (u32x2){0x00003f80u, 0u};
  if (tid < 256) *(LAS u32x2*)(smw + KVT + (tid >> 2) * 128 + (tid & 3) * 32 + 24) = (u32x2){0u, 0u};
  LDS_BAR();
  STAMP(a.ws, 0, 3);
  const rsrc_t rkv = mk_rsrc(a.ws + WS_KVP + (size_t)j * 8 * KVP_STRIDE, 8 * KVP_STRIDE);
  if (w < 6) {
    const int uh = w / 3, mt = w - uh * 3;
    f32x4 d0 = z4, d1 = z4;
#pragma unroll
    for (int ks = 0; ks < 8; ++ks) {
      bf16x4 av[2], b0[2], b1[2];
#pragma unroll
      for (int u = 0; u < 2; ++u) {
        const LAS char* rp = sm + (32 * ks + 8 * q + 4 * u + (r16 >> 2)) * IROW;
        av[u] = ld_tr(rp + uh * 96 + mt * 32 + (r16 & 3) * 8);
        b0[u] = ld_tr(rp + 192 + uh * 40 + (r16 & 3) * 8);
        b1[u] = ld_tr((r16 & 3) == 0 ? rp + 192 + uh * 40 + 32 : sm + ONES);
      }
      const bf16x8 A = cat(av[0], av[1]);
      d0 = mfma16(A, cat(b0[0], b0[1]), d0);
      d1 = mfma16(A, cat(b1[0], b1[1]), d1);
    }
    const unsigned base = (unsigned)(p * KVP_STRIDE + (uh * 3 + mt) * 1344);
    __builtin_amdgcn_raw_buffer_store_b128((u32x4){pk2(d0[0], d0[1]), tag, pk2(d0[2], d0[3]), tag}, rkv, base + lane * 16, 0, SC1);
    __builtin_amdgcn_raw_buffer_store_b128((u32x4){pk2(d1[0], d1[1]), tag, pk2(d1[2], d1[3]), tag}, rkv, r16 < 5 ? base + 1024 + (q * 5 + r16) * 16 : OOB, 0, SC1);
  }
  STAMP(a.ws, 0, 14);
  u32x4 Fq0[4]; unsigned Fq1[4];
  {
    float L[4][10];
    dual_softmax4(dq1, dq2, L);
#pragma unroll
    for (int rt = 0; rt < 4; ++rt) {
      Fq0[rt] = (u32x4){pk2(L[rt][0], L[rt][1]), pk2(L[rt][2], L[rt][3]), pk2(L[rt][4], L[rt][5]), pk2(L[rt][6], L[rt][7])};
      Fq1[rt] = pk2(L[rt][8], L[rt][9]);
    }
  }
  STAMP(a.ws, 0, 4);
  if (tid < 504) {
    u32x4 v[8];
    for (unsigned spins = 0; spins < SPIN_CAP; ++spins) {
      bool ok = true;
#pragma unroll
      for (int k = 0; k < 8; ++k) v[k] = __builtin_amdgcn_raw_buffer_load_b128(rkv, (unsigned)(tid * 16), k * KVP_STRIDE, SC1);
#pragma unroll
      for (int k = 0; k < 8; ++k) ok &= (v[k][1] == tag) & (v[k][3] == tag);
      if (__all(ok)) break;
      __builtin_amdgcn_s_sleep(1);
    }
    f32x4 s = z4;
#pragma unroll
    for (int k = 0; k < 8; ++k) { const unsigned lo = v[k][0], hi = v[k][2]; s += (f32x4){uf(lo << 16), uf(lo & 0xffff0000u), uf(hi << 16), uf(hi & 0xffff0000u)}; }
    const int un = tid / 84, o = tid - un * 84, uh = un / 3, mt = un - uh * 3;
    int qq, dc;
    if (o < 64) { qq = o >> 4; dc = o & 15; } else { const int o2 = o - 64; qq = o2 / 5; dc = 16 + (o2 - qq * 5); }
    const int phi = 16 * mt + 4 * qq, lq = phi / 12, m = phi - 12 * lq;
    *(LAS u32x2*)(smw + KVT + (uh * 32 + dc) * 128 + lq * 32 + m * 2) = (u32x2){pk2(s[0], s[1]), pk2(s[2], s[3])};
  }
  LDS_BAR();
  STAMP(a.ws, 0, 6);
  {
    bf16x8 At[2][2];
#pragma unroll
    for (int mt = 0; mt < 2; ++mt)
#pragma unroll
      for (int s = 0; s < 2; ++s) At[mt][s] = ld_b128(sm + KVT + (hd * 32 + mt * 16 + r16) * 128 + q * 32 + s * 16);
    const rsrc_t rafr = mk_rsrc(a.ws + WS_AFR, (unsigned)AFR_BYTES);
    const int H = 2 * j + hd;
    const int sA = 20 * H + 4 * q, sB = 20 * H + 16;
    const unsigned oA = (unsigned)((sA >> 5) * 1024 + (((sA & 31) >> 3) * 16 + r16) * 16 + ((sA >> 2) & 1) * 8);
    const unsigned oB = (unsigned)((sB >> 5) * 1024 + (((sB & 31) >> 3) * 16 + r16) * 16 + ((sB >> 2) & 1) * 8);
#pragma unroll
    for (int rt = 0; rt < 4; ++rt) {
      const bf16x8 B0 = __builtin_bit_cast(bf16x8, Fq0[rt]), B1 = __builtin_bit_cast(bf16x8, (u32x4){Fq1[rt], 0u, 0u, 0u});
      f32x4 d0 = mfma16(At[0][0], B0, z4); d0 = mfma16(At[0][1], B1, d0);
      f32x4 d1 = mfma16(At[1][0], B0, z4); d1 = mfma16(At[1][1], B1, d1);
      const float inv = __builtin_amdgcn_rcpf(bcast_q1(d1[0]) + 1e-6f);
      const unsigned base = (unsigned)((p * 16 + rq * 4 + rt) * KS * 1024);
      __builtin_amdgcn_raw_buffer_store_b64((u32x2){pk2(d0[0] * inv, d0[1] * inv), pk2(d0[2] * inv, d0[3] * inv)}, rafr, base + oA, 0, SC1);
      if (q == 0) __builtin_amdgcn_raw_buffer_store_b64((u32x2){pk2(d1[0] * inv, d1[1] * inv), pk2(d1[2] * inv, d1[3] * inv)}, rafr, base + oB, 0, SC1);
    }
  }
  STAMP(a.ws, 0, 7);
}

constexpr int NH = 12, NO = 5, NS2 = NH + NO;
constexpr int NPRE = 4;
constexpr int G2_PRE = 81920;
constexpr int G2_FLAG = G2_PRE - 64;
DI int g2_ks(int w, int s) { return s >= NH ? 5 * w + (s - NH) : 20 + NH * w + s; }
DI void g2_preload_piece(const Args& a, unsigned lds0, int b, int lane, int f) {
  const int ct80 = (b >> 3) & 7, tile = f / 5, i = f - tile * 5, w4 = tile / NPRE, s = tile - w4 * NPRE;
  const rsrc_t r = mk_rsrc(a.wd, 1536 * 1280);
  const int P = i * 64 + lane, row = P / 10, pc = P - row * 10;
  dma16(lds0 + G2_PRE + f * 1024, (unsigned)(row * 1280 + pc * 16), r, (unsigned)((NH * w4 + s) * 32 * 1280 + ct80 * 160));
}
DI void gemm2_body(const Args& a, char* smem, int w, int lane, int b, unsigned tag) {
  const int tid = w * 64 + lane;
  const int xcd = b & 7, idx = b >> 3;
  const int rt64 = xcd * 4 + (idx >> 3), ct80 = idx & 7;
  const LAS char* sm = (const LAS char*)smem;
  volatile LAS unsigned* gflag = (volatile LAS unsigned*)(smem + G2_FLAG);
  u32x4 xr[3];
  LAS float* red = (LAS float*)smem;
  f32x4 acc[5][4];
  if (w < 4) {
    const rsrc_t ra = mk_rsrc(a.ws + WS_AFR + (size_t)rt64 * 4 * KS * 1024, 4 * KS * 1024);
    const rsrc_t rb = mk_rsrc(a.ws + WS_BFR + (size_t)ct80 * 5 * KS * 1024, 5 * KS * 1024);
    constexpr int DA = 4, DB = 3;
    u32x4 A[DA][4], B[DB][5];
    bf16x8 BL[3];
    const unsigned voff = lane * 16;
    auto ldB = [&](int s, int c) { B[s % DB][c] = __builtin_amdgcn_raw_buffer_load_b128(rb, voff, (unsigned)(g2_ks(w, s) + c * KS) * 1024u, SC1); };
    auto ldA = [&](int s, int r) { A[s % DA][r] = __builtin_amdgcn_raw_buffer_load_b128(ra, voff, (unsigned)(g2_ks(w, s) + r * KS) * 1024u, SC1); };
    const LAS char* pre = sm + G2_PRE + w * NPRE * 5120 + (4 * (lane >> 4) + ((lane & 15) >> 2)) * 160 + (lane & 3) * 8;
    auto rdB = [&](int f) { const LAS char* pp = pre + (f / 5) * 5120 + (f % 5) * 32; BL[f % 3] = cat(ld_tr(pp), ld_tr(pp + 16 * 160)); };
#pragma unroll
    for (int s = 0; s < DA - 1; ++s)
#pragma unroll
      for (int r = 0; r < 4; ++r) ldA(s, r);
    rdB(0); rdB(1);
#pragma unroll
    for (int c = 0; c < 5; ++c)
#pragma unroll
      for (int r = 0; r < 4; ++r) acc[c][r] = (f32x4){0.f, 0.f, 0.f, 0.f};
    SB();
    unsigned long long ps[20]; (void)ps;
    PSTAMP2(ps, 0);
#pragma unroll
    for (int s = 0; s < NS2; ++s) {
      if (s == NH - DA + 1) {
        for (unsigned spins = 0; spins < SPIN_CAP * 8; ++spins) { if (*gflag == 1u) break; __builtin_amdgcn_s_sleep(1); }
        SB();
      }
#pragma unroll
      for (int c = 0; c < 5; ++c) {
        if (s * 5 + c + 2 < NPRE * 5) rdB(s * 5 + c + 2);
        bf16x8 Bc;
        if (s < NPRE) Bc = BL[(s * 5 + c) % 3];
        else Bc = __builtin_bit_cast(bf16x8, B[s % DB][c]);
#pragma unroll
        for (int r = 0; r < 4; ++r) acc[c][r] = mfma16(Bc, __builtin_bit_cast(bf16x8, A[s % DA][r]), acc[c][r]);
        SB();
        if (c < 4 && s - 1 + DA < NS2) ldA(s - 1 + DA, c);
        if (s + DB >= NPRE && s + DB < NS2) ldB(s + DB, c);
        SB();
      }
      PSTAMP2(ps, s + 1);
    }
    PDUMP2(a.ws, ps, 18);
    STAMP(a.ws, 1, 2);
  } else {
    const rsrc_t ra = mk_rsrc(a.ws + WS_AFR + (size_t)rt64 * 4 * KS * 1024, 4 * KS * 1024);
    const unsigned dummy = (unsigned)(uintptr_t)sm + G2_FLAG - 256;
    if (w < 7) { const int l = ct80 * 192 + (w - 4) * 64 + lane, rt = l / 384, rem = l - rt * 384; touch_line(dummy, (unsigned)((rt * KS + 20) * 1024 + rem * 128), ra, 0); }
#pragma unroll
    for (int i = 0; i < 3; ++i) {
      const int it = tid - 256 + 256 * i;
      if (it < 640) { const int row = it / 10, g = it - row * 10; xr[i] = *(const u32x4*)(a.x + (size_t)(rt64 * 64 + row) * 640 + ct80 * 80 + g * 8); }
    }
    if (w == 7) {
      unsigned fq[2];
      flags_load<2>(a, 0, lane, fq);
      flags_wait<2>(a, 0, lane, fq, tag);
      if (b == 0 && lane == 0) __hip_atomic_store((GAS unsigned*)(a.ws + WS_EPOCH), tag, RLX_AGENT);
#pragma unroll
      for (int i = 0; i < 2; ++i) { const int l = ct80 * 80 + i * 64 + lane, rt = l / 160, rem = l - rt * 160; touch_line(dummy, i * 64 + lane < 80 ? (unsigned)(rt * KS * 1024 + rem * 128) : OOB, ra, 0); }
      if (lane == 0) *gflag = 1u;
    }
  }
  LDS_BAR();
  if (w < 4) {
#pragma unroll
    for (int c = 0; c < 5; ++c)
#pragma unroll
      for (int r = 0; r < 4; ++r) *(LAS f32x4*)(red + (w * 64 + r * 16 + (lane & 15)) * 84 + c * 16 + (lane >> 4) * 4) = acc[c][r];
  }
  LDS_BAR();
  if (w >= 4)
#pragma unroll
  for (int i = 0; i < 3; ++i) {
    const int it = tid - 256 + 256 * i;
    if (it < 640) {
      const int row = it / 10, g = it - row * 10;
      f32x4 s0 = {0.f, 0.f, 0.f, 0.f}, s1 = {0.f, 0.f, 0.f, 0.f};
#pragma unroll
      for (int k = 0; k < 4; ++k) { s0 += *(LAS f32x4*)(red + (k * 64 + row) * 84 + g * 8); s1 += *(LAS f32x4*)(red + (k * 64 + row) * 84 + g * 8 + 4); }
      u32x4 o;
#pragma unroll
      for (int e = 0; e < 4; ++e) {
        const unsigned xe = xr[i][e];
        const float v0 = e < 2 ? s0[2 * e] : s1[2 * e - 4], v1 = e < 2 ? s0[2 * e + 1] : s1[2 * e - 3];
        o[e] = pk2(uf(xe << 16) + v0, uf(xe & 0xffff0000u) + v1);
      }
      *(u32x4*)(a.out + (size_t)(rt64 * 64 + row) * 640 + ct80 * 80 + g * 8) = o;
    }
  }
}

__global__ __launch_bounds__(512, 2) void fused_kernel(Args a) {
  extern __shared__ __attribute__((aligned(16))) char smem[];
  const int tid = threadIdx.x, lane = tid & 63, w = rfl(tid >> 6);
  const int b = blockIdx.x, xcd = b & 7, idx = b >> 3;
  const int p = idx & 7, jj = (idx >> 3) & 1, j = xcd * 2 + jj;
  STAMP(a.ws, 0, 0);
  unsigned tag;
  dma4_sc1((unsigned)(uintptr_t)(LAS char*)smem + SCR_TAG + w * 256, 0u, mk_rsrc(a.ws + WS_EPOCH, 64));
  const int off = p == 7 ? 5 : 0;
  if (idx < 16) qkv_body(a, smem, p, j, w, lane, b, off, tag);
  else glu_body(a, smem, p, j, w, lane, b, off, tag);
  STAMP(a.ws, 1, 3);
  unsigned fg[2];
  if (w == 1) flags_load<2>(a, 128, lane, fg);
  WAIT_BAR(0);
  if (tid == 0) {
    __hip_atomic_store((GAS unsigned*)(a.ws + WS_DONE) + b * 4, tag, RLX_AGENT);
    *(volatile LAS unsigned*)((LAS char*)smem + G2_FLAG) = 0u;
  }
  STAMP(a.ws, 1, 4);
  if (w == 1) flags_wait<2>(a, 128, lane, fg, tag);
  WAIT_BAR(0);
  STAMP(a.ws, 1, 0);
  gemm2_body(a, smem, w, lane, b, tag);
  STAMP(a.ws, 1, 7);
}

extern "C" void kernel_launch(void* const* d_in, const int* in_sizes, int n_in, void* d_out, int out_size, void* d_ws, size_t ws_size, hipStream_t stream) {
  (void)in_sizes; (void)n_in; (void)out_size; (void)ws_size;
  Args a;
  a.x = (const uint16_t*)d_in[0]; a.wqkv = (const uint16_t*)d_in[1]; a.wphi = (const uint16_t*)d_in[2]; a.wo = (const uint16_t*)d_in[3];
  a.wg = (const uint16_t*)d_in[5]; a.wu = (const uint16_t*)d_in[6]; a.wd = (const uint16_t*)d_in[7]; a.ws = (char*)d_ws; a.out = (uint16_t*)d_out;
  (void)hipFuncSetAttribute((const void*)fused_kernel, hipFuncAttributeMaxDynamicSharedMemorySize, LDS_BYTES);
  fused_kernel<<<256, 512, LDS_BYTES, stream>>>(a);
}
```

```cpp
#include <hip/hip_runtime.h>
#include <stdint.h>

typedef __bf16 bf16x2 __attribute__((ext_vector_type(2)));
typedef __bf16 bf16x4 __attribute__((ext_vector_type(4)));
typedef __bf16 bf16x8 __attribute__((ext_vector_type(8)));
typedef float f32x2 __attribute__((ext_vector_type(2)));
typedef float f32x4 __attribute__((ext_vector_type(4)));
typedef unsigned u32x2 __attribute__((ext_vector_type(2)));
typedef unsigned u32x4 __attribute__((ext_vector_type(4)));
#define LAS __attribute__((address_space(3)))
#define GAS __attribute__((address_space(1)))
#define DI __device__ __forceinline__
typedef __amdgpu_buffer_rsrc_t rsrc_t;
#define RLX_AGENT __ATOMIC_RELAXED, __HIP_MEMORY_SCOPE_AGENT

constexpr int KS = 68;
constexpr size_t WS_EPOCH = 0;
constexpr size_t WS_RL = 16384;
constexpr size_t WS_DONE = 20480;
constexpr size_t WS_STAT = 32768;
constexpr size_t WS_CS = 65536;
constexpr int CS_TILE = 192 * 8;
constexpr size_t WS_KVP = 1u << 20;
constexpr int KVP_STRIDE = 8192;
constexpr size_t WS_AFR = 2u << 20;
constexpr size_t AFR_BYTES = (size_t)128 * KS * 1024;
constexpr size_t WS_BFR = WS_AFR + AFR_BYTES;
constexpr size_t BFR_BYTES = (size_t)40 * KS * 1024;

constexpr float LOG2E = 1.4426950408889634f;
constexpr int SC1 = 16;
constexpr unsigned OOB = 0x7ffffff0u;
constexpr unsigned SPIN_CAP = 1u << 18;

DI rsrc_t mk_rsrc(const void* p, unsigned bytes) { return __builtin_amdgcn_make_buffer_rsrc((void*)p, (short)0, (int)bytes, 0x00020000); }
DI unsigned rfl(unsigned v) { return __builtin_amdgcn_readfirstlane(v); }
DI void dma16(unsigned lds_addr, unsigned voff, rsrc_t r, unsigned soff) {
  asm volatile("s_mov_b32 m0, %0\n\ts_nop 0\n\tbuffer_load_dwordx4 %1, %2, %3 offen lds" ::"s"(lds_addr), "v"(voff), "s"(r), "s"(soff) : "m0", "memory");
}
DI void set_m0(unsigned lds_addr) { asm volatile("s_mov_b32 m0, %0" ::"s"(lds_addr) : "m0", "memory"); }
template <int OFF>
DI void dma16_off(unsigned voff, rsrc_t r, unsigned soff) {
  asm volatile("buffer_load_dwordx4 %0, %1, %2 offen offset:%3 lds" ::"v"(voff), "s"(r), "s"(soff), "i"(OFF) : "memory");
}
DI void dma16_i(int i, unsigned voff, rsrc_t r, unsigned soff) {
  if (i == 0) dma16_off<0>(voff, r, soff); else if (i == 1) dma16_off<1024>(voff, r, soff); else if (i == 2) dma16_off<2048>(voff, r, soff); else dma16_off<3072>(voff, r, soff);
}
DI void dma4_sc1(unsigned lds_addr, unsigned voff, rsrc_t r) {
  asm volatile("s_mov_b32 m0, %0\n\ts_nop 0\n\tbuffer_load_dword %1, %2, 0 offen sc1 lds" ::"s"(lds_addr), "v"(voff), "s"(r) : "m0", "memory");
}
DI void touch_line(unsigned lds_addr, unsigned voff, rsrc_t r, unsigned soff) {
  asm volatile("s_mov_b32 m0, %0\n\ts_nop 0\n\tbuffer_load_dword %1, %2, %3 offen lds" ::"s"(lds_addr), "v"(voff), "s"(r), "s"(soff) : "m0", "memory");
}
#define WAIT_BAR(N) asm volatile("s_waitcnt vmcnt(" #N ") lgkmcnt(0)\n\ts_barrier" ::: "memory")
#define WAIT_VM(N) asm volatile("s_waitcnt vmcnt(" #N ")" ::: "memory")
#define LDS_BAR() asm volatile("s_waitcnt lgkmcnt(0)\n\ts_barrier" ::: "memory")
#define LDS_WAIT() asm volatile("s_waitcnt lgkmcnt(0)" ::: "memory")
#define SB() __builtin_amdgcn_sched_barrier(0)
DI void mfma_fence(f32x4& a) { asm volatile("s_nop 15" : "+v"(a)); }
DI void mfma_fence(f32x4& a, f32x4& b) { asm volatile("s_nop 15" : "+v"(a), "+v"(b)); }
DI void mfma_fence(f32x4 (&a)[4], f32x4 (&b)[4]) { asm volatile("s_nop 15" : "+v"(a[0]), "+v"(a[1]), "+v"(a[2]), "+v"(a[3]), "+v"(b[0]), "+v"(b[1]), "+v"(b[2]), "+v"(b[3])); }
#define PIN1(x) asm volatile("" : "+v"(x))

DI bf16x8 ld_b128(const LAS char* p) { return *(const LAS bf16x8*)p; }
DI bf16x4 ld_tr(const LAS char* p) { return __builtin_amdgcn_ds_read_tr16_b64_v4bf16((LAS bf16x4*)p); }
DI bf16x8 cat(bf16x4 a, bf16x4 b) { return __builtin_shufflevector(a, b, 0, 1, 2, 3, 4, 5, 6, 7); }
DI f32x4 mfma16(bf16x8 a, bf16x8 b, f32x4 c) { return __builtin_amdgcn_mfma_f32_16x16x32_bf16(a, b, c, 0, 0, 0); }
DI bf16x8 ones_if(bool c) { const unsigned v = c ? 0x3f803f80u : 0u; return __builtin_bit_cast(bf16x8, (u32x4){v, v, v, v}); }
DI unsigned pk2(float a, float b) { bf16x2 p; p[0] = (__bf16)a; p[1] = (__bf16)b; return __builtin_bit_cast(unsigned, p); }
DI unsigned fu(float x) { return __float_as_uint(x); }
DI float uf(unsigned x) { return __uint_as_float(x); }
DI float q_add(float v) { auto a = __builtin_amdgcn_permlane16_swap(fu(v), fu(v), false, false); v = uf(a[0]) + uf(a[1]); auto b = __builtin_amdgcn_permlane32_swap(fu(v), fu(v), false, false); return uf(b[0]) + uf(b[1]); }
DI float mx3(float a, float b, float c) { float r; asm("v_max3_f32 %0, %1, %2, %3\n\ts_nop 1" : "=v"(r) : "v"(a), "v"(b), "v"(c)); return r; }
DI float mn3(float a, float b, float c) { float r; asm("v_min3_f32 %0, %1, %2, %3\n\ts_nop 1" : "=v"(r) : "v"(a), "v"(b), "v"(c)); return r; }
DI float mx3i(float a, float b, float c) { float r; asm("v_max3_f32 %0, %1, %2, %3" : "=v"(r) : "v"(a), "v"(b), "v"(c)); return r; }
DI float mn3i(float a, float b, float c) { float r; asm("v_min3_f32 %0, %1, %2, %3" : "=v"(r) : "v"(a), "v"(b), "v"(c)); return r; }
DI float q_max(float v) { auto a = __builtin_amdgcn_permlane16_swap(fu(v), fu(v), false, false); v = mx3(uf(a[0]), uf(a[1]), uf(a[1])); auto b = __builtin_amdgcn_permlane32_swap(fu(v), fu(v), false, false); return mx3i(uf(b[0]), uf(b[1]), uf(b[1])); }
DI float q_min(float v) { auto a = __builtin_amdgcn_permlane16_swap(fu(v), fu(v), false, false); v = mn3(uf(a[0]), uf(a[1]), uf(a[1])); auto b = __builtin_amdgcn_permlane32_swap(fu(v), fu(v), false, false); return mn3i(uf(b[0]), uf(b[1]), uf(b[1])); }
DI float row_add(float v) {
  v += uf(__builtin_amdgcn_update_dpp(0u, fu(v), 0x128, 0xf, 0xf, false));
  v += uf(__builtin_amdgcn_update_dpp(0u, fu(v), 0x124, 0xf, 0xf, false));
  v += uf(__builtin_amdgcn_update_dpp(0u, fu(v), 0x122, 0xf, 0xf, false));
  v += uf(__builtin_amdgcn_update_dpp(0u, fu(v), 0x121, 0xf, 0xf, false));
  return v;
}
DI float bcast_q1(float v) { auto a = __builtin_amdgcn_permlane16_swap(fu(v), fu(v), false, false); auto b = __builtin_amdgcn_permlane32_swap(a[1], a[1], false, false); return uf(b[0]); }

constexpr int XS_SLOT = 32768;
constexpr int WS_OFF = 3 * XS_SLOT;
constexpr int WG_SLOT = 24576, WG_U = 12288;
constexpr int WQ_SLOT = 16384;
constexpr int GL_MISC = WS_OFF + 2 * WG_SLOT;
constexpr int QK_PHI = WS_OFF + 2 * WQ_SLOT;
constexpr int QK_MISC = QK_PHI + 8192;
constexpr int QK_RAW = QK_MISC + 2560;
constexpr int SCR_ROW = 150528;
constexpr int SCR_TAG = SCR_ROW + 10240;
constexpr int SCR_DUMMY = SCR_TAG + 2048;
constexpr int SCR_SINK = 148480;
constexpr int G_CS = 150528, G_ST = G_CS + 9216;
constexpr int G2_FLAG_C = 81920 - 512;
constexpr int LDS_BYTES = 160 * 1024;

struct Args { const uint16_t *x, *wqkv, *wphi, *wo, *wg, *wu, *wd; char* ws; uint16_t* out; };

DI int rot10(int t, int off) { const int v = t + off; return v >= 10 ? v - 10 : v; }

constexpr int N_UNITS = 520;
DI int unit_ks(int unit) { const int i = unit / 10; return i < 20 ? i : 20 + ((i - 20) >> 3) * 12 + 4 + ((i - 20) & 7); }
DI void relayout_issue(const Args& a, int unit, unsigned scratch, int lane) {
  const int ks = unit_ks(unit), cg = unit % 10;
  const rsrc_t r = ks < 20 ? mk_rsrc(a.wo, 640 * 640 * 2) : mk_rsrc(a.wd, 1536 * 640 * 2);
  const unsigned row0 = ks < 20 ? ks * 32 : (ks - 20) * 32;
  const unsigned voff = (lane >> 3) * 1280 + (lane & 7) * 16;
#pragma unroll
  for (int i = 0; i < 4; ++i) dma16(scratch + i * 1024, voff, r, (row0 + 8 * i) * 1280 + cg * 128);
}
DI void relayout_touch(const Args& a, int unit, unsigned dummy, int lane) {
  const int ks = unit_ks(unit < N_UNITS ? unit : 0), cg = unit % 10;
  const rsrc_t r = ks < 20 ? mk_rsrc(a.wo, 640 * 640 * 2) : mk_rsrc(a.wd, 1536 * 640 * 2);
  const unsigned row0 = ks < 20 ? ks * 32 : (ks - 20) * 32;
  touch_line(dummy, (lane < 32 && unit < N_UNITS) ? (row0 + lane) * 1280 + cg * 128 : OOB, r, 0);
}
DI void relayout_finish(const Args& a, int unit, const LAS char* sc, int lane) {
  const int ks = unit_ks(unit), cg = unit % 10;
  const int q = lane >> 4, i = lane & 15;
  const rsrc_t rb = mk_rsrc(a.ws + WS_BFR, (unsigned)BFR_BYTES);
  const unsigned dst = ((cg * 4) * KS + ks) * 1024 + lane * 16;
#pragma unroll
  for (int c4 = 0; c4 < 4; ++c4) {
    bf16x4 v[2];
#pragma unroll
    for (int u = 0; u < 2; ++u) {
      const int rb0 = ks < 20 ? 8 * q + 4 * u : 16 * u + 4 * q;
      v[u] = ld_tr(sc + (rb0 + (i >> 2)) * 128 + c4 * 32 + (i & 3) * 8);
    }
    __builtin_amdgcn_raw_buffer_store_b128(__builtin_bit_cast(u32x4, cat(v[0], v[1])), rb, dst, c4 * KS * 1024, SC1);
  }
}
DI void rowstat_issue(const Args& a, int row, unsigned scratch, int lane) {
  const rsrc_t r = mk_rsrc(a.x + (size_t)row * 640, 1280);
  dma16(scratch, lane * 16, r, 0);
  if (lane < 16) dma16(scratch + 1024, lane * 16, r, 1024);
}
struct RowStat { bf16x8 v0, v1; f32x4 s, q; float s1, q1; };
DI void rowstat_a(RowStat& r, const LAS char* sc, int lane) {
  r.v0 = ld_b128(sc + lane * 16);
  const u32x4 t = *(const LAS u32x4*)(sc + 1024 + (lane & 15) * 16);
  const unsigned m = lane < 16 ? ~0u : 0u;
  r.v1 = __builtin_bit_cast(bf16x8, t & (u32x4){m, m, m, m});
}
DI void rowstat_b(RowStat& r) {
  const f32x4 z = {0.f, 0.f, 0.f, 0.f};
  const bf16x8 one = ones_if(true);
  r.s = mfma16(r.v1, one, mfma16(r.v0, one, z));
  r.q = mfma16(r.v1, r.v1, mfma16(r.v0, r.v0, z));
}
DI void rowstat_c(RowStat& r, int lane) {
  mfma_fence(r.s, r.q);
  r.s1 = (r.s[0] + r.s[1]) + (r.s[2] + r.s[3]);
  const int e = lane & 3;
  const float d = e == 0 ? r.q[0] : e == 1 ? r.q[1] : e == 2 ? r.q[2] : r.q[3];
  r.q1 = row_add(((lane >> 4) == ((lane & 15) >> 2)) ? d : 0.f);
  asm volatile("" : "+v"(r.s1), "+v"(r.q1));
}
DI void rowstat_d(const Args& a, RowStat& r, int row, int lane, unsigned tag) {
  const float s = q_add(r.s1), q = q_add(r.q1);
  const float mean = s * (1.f / 640.f);
  const float rstd = rsqrtf(fmaxf(q * (1.f / 640.f) - mean * mean, 0.f) + 1e-5f);
  const rsrc_t rs = mk_rsrc(a.ws + WS_STAT, 2048 * 16);
  const u32x4 g = {fu(mean), tag, fu(rstd), tag};
  __builtin_amdgcn_raw_buffer_store_b128(g, rs, lane == 0 ? (unsigned)(row * 16) : OOB, 0, SC1);
}

DI void x_offsets(int w, int lane, unsigned (&vx)[4]) {
#pragma unroll
  for (int i = 0; i < 4; ++i) vx[i] = (w * 32 + 8 * i + (lane >> 3)) * 1280 + (((lane & 7) ^ ((lane >> 4) | (4 * (i & 1)))) * 16) - i * 1024;
}
DI void x_m0(unsigned lds0, int t, int w) { set_m0(lds0 + (t % 3) * XS_SLOT + w * 4096); }
DI void x_tile(const rsrc_t rx, unsigned lds0, int t, int off, int w, const unsigned (&vx)[4]) {
  x_m0(lds0, t, w);
  asm volatile("s_nop 0");
  const unsigned so = rot10(t, off) * 128;
#pragma unroll
  for (int i = 0; i < 4; ++i) dma16_i(i, vx[i], rx, so);
}

DI void dma16_sc1(unsigned lds_addr, unsigned voff, rsrc_t r, unsigned soff) {
  asm volatile("s_mov_b32 m0, %0\n\ts_nop 0\n\tbuffer_load_dwordx4 %1, %2, %3 offen sc1 lds" ::"s"(lds_addr), "v"(voff), "s"(r), "s"(soff) : "m0", "memory");
}
template <int NCOL>
DI void gather_piece(const Args& a, int kind, int j, int p, unsigned lds0, int lane, int i) {
  constexpr int NC = NCOL * 6 * 8 / 1024;
  const bool isc = i < NC;
  const char* base = isc ? a.ws + WS_CS + (size_t)(kind * 16 + j) * 6 * CS_TILE : a.ws + WS_STAT + (size_t)p * 256 * 16;
  const rsrc_t r = mk_rsrc(base, isc ? 6 * CS_TILE : (i < NC + 4 ? 256 * 16 : 0));
  const unsigned dst = isc ? G_CS + i * 1024 : (i < NC + 4 ? G_ST + (i - NC) * 1024 : SCR_SINK);
  const unsigned so = isc ? (NCOL == 192 ? i * 1024 : i * CS_TILE) : (i - NC) * 1024;
  dma16_sc1(lds0 + dst, lane * 16, r, so);
}
template <int NCOL>
DI void gather_finish(const Args& a, int kind, int j, int p, int tid, const LAS char* sm, unsigned tag, LAS float* cs, LAS float* st) {
  if (tid < NCOL) {
    u32x2 g[6];
#pragma unroll
    for (int t = 0; t < 6; ++t) g[t] = *(const LAS u32x2*)(sm + G_CS + (t * NCOL + tid) * 8);
    for (unsigned spins = 0; spins < SPIN_CAP; ++spins) {
      bool ok = true;
#pragma unroll
      for (int t = 0; t < 6; ++t) ok &= g[t][1] == tag;
      if (__all(ok)) break;
      __builtin_amdgcn_s_sleep(2);
      const rsrc_t rc = mk_rsrc(a.ws + WS_CS + (size_t)(kind * 16 + j) * 6 * CS_TILE, 6 * CS_TILE);
#pragma unroll
      for (int t = 0; t < 6; ++t) g[t] = __builtin_amdgcn_raw_buffer_load_b64(rc, tid * 8, t * CS_TILE, SC1);
    }
    cs[tid] = ((uf(g[0][0]) + uf(g[1][0])) + (uf(g[2][0]) + uf(g[3][0]))) + (uf(g[4][0]) + uf(g[5][0]));
  }
  if (tid >= 256) {
    u32x4 g = *(const LAS u32x4*)(sm + G_ST + (tid - 256) * 16);
    for (unsigned spins = 0; spins < SPIN_CAP; ++spins) {
      if (__all((g[1] == tag) & (g[3] == tag))) break;
      __builtin_amdgcn_s_sleep(2);
      const rsrc_t rs = mk_rsrc(a.ws + WS_STAT + (size_t)p * 256 * 16, 256 * 16);
      g = __builtin_amdgcn_raw_buffer_load_b128(rs, (tid - 256) * 16, 0, SC1);
    }
    const f32x2 v = {uf(g[2]), -uf(g[0])};
    *(LAS f32x2*)(st + (tid - 256) * 2) = v;
  }
}
template <int NCOL>
DI bool gather_fast(int tid, const LAS char* sm, unsigned tag, LAS float* cs, LAS float* st) {
  const int col = NCOL == 128 ? tid & 127 : (tid < 384 ? (tid >= 192 ? tid - 192 : tid) : tid - 384), row = tid & 255;
  u32x2 g[6];
#pragma unroll
  for (int t = 0; t < 6; ++t) g[t] = *(const LAS u32x2*)(sm + G_CS + (t * NCOL + col) * 8);
  const u32x4 h = *(const LAS u32x4*)(sm + G_ST + row * 16);
  bool ok = (h[1] == tag) & (h[3] == tag);
#pragma unroll
  for (int t = 0; t < 6; ++t) ok &= g[t][1] == tag;
  cs[tid < NCOL ? col : NCOL + 512 + 2] = ((uf(g[0][0]) + uf(g[1][0])) + (uf(g[2][0]) + uf(g[3][0]))) + (uf(g[4][0]) + uf(g[5][0]));
  *(LAS f32x2*)(st + (tid >= 256 ? row * 2 : 512)) = (f32x2){uf(h[2]), -uf(h[0])};
  return ok;
}
DI bool cs_owner(int p, int t) { return t < 5 && (p & 3) == t % 3; }
DI void cs_publish(const Args& a, int kind, int j, int p, int col0, f32x4 s, int lane, unsigned tag) {
  mfma_fence(s);
  const rsrc_t rc = mk_rsrc(a.ws + WS_CS + (size_t)(kind * 16 + j) * 6 * CS_TILE, 6 * CS_TILE);
  const bool on = (lane & 15) == 0 && col0 >= 0 && (p & 3) < 3;
  const unsigned o = on ? (unsigned)((col0 + 4 * (lane >> 4)) * 8 + ((p >> 2) * 3 + (p & 3)) * CS_TILE) : OOB;
  __builtin_amdgcn_raw_buffer_store_b128((u32x4){fu(s[0]), tag, fu(s[1]), tag}, rc, o, 0, SC1);
  __builtin_amdgcn_raw_buffer_store_b128((u32x4){fu(s[2]), tag, fu(s[3]), tag}, rc, on ? o + 16 : OOB, 0, SC1);
}
DI bool panel_ok(const Args& a, int kind, int x, int lane, unsigned tag) {
  GAS unsigned* d = (GAS unsigned*)(a.ws + WS_DONE);
  GAS unsigned* r = (GAS unsigned*)(a.ws + WS_RL);
  const int bb = (kind * 16 + x + 8 * ((lane >> 3) & 1)) * 8 + (lane & 7);
  const unsigned v = __hip_atomic_load(d + bb * 4, RLX_AGENT);
  bool ok = v == tag;
  if (kind == 1) { const unsigned r0 = __hip_atomic_load(r + lane, RLX_AGENT), r1 = __hip_atomic_load(r + 64 + lane, RLX_AGENT); ok &= (r0 == tag) & (r1 == tag); }
  return __all(ok);
}
template <int N>
DI void flags_load(const Args& a, int first, int lane, unsigned (&v)[N]) {
  GAS unsigned* f = (GAS unsigned*)(a.ws + WS_DONE);
#pragma unroll
  for (int i = 0; i < N; ++i) v[i] = __hip_atomic_load(f + (first + i * 64 + lane) * 4, RLX_AGENT);
}
template <int N>
DI void flags_wait(const Args& a, int first, int lane, unsigned (&v)[N], unsigned tag) {
  for (unsigned spins = 0; spins < SPIN_CAP; ++spins) {
    bool ok = true;
#pragma unroll
    for (int i = 0; i < N; ++i) ok &= v[i] == tag;
    if (__all(ok)) break;
    __builtin_amdgcn_s_sleep(1);
    flags_load<N>(a, first, lane, v);
  }
}

DI void g2_preload_piece(const Args& a, unsigned lds0, int b, int lane, int f);
DI void glu_body(const Args& a, char* smem, int p, int j, int w, int lane, int b, int off, unsigned& tag) {
  const LAS char* sm = (const LAS char*)smem;
  const unsigned lds0 = (unsigned)(uintptr_t)sm;
  const int r16 = lane & 15, q = lane >> 4, tid = w * 64 + lane;
  const int rq = w >> 1, ch = w & 1;
  const rsrc_t rx = mk_rsrc(a.x + (size_t)p * 256 * 640, 256 * 1280);
  const rsrc_t rw = mk_rsrc((w < 4 ? a.wg : a.wu), 640 * 3072);
  unsigned vx[4];
  x_offsets(w, lane, vx);
  unsigned vw[3];
#pragma unroll
  for (int i = 0; i < 3; ++i) {
    const int P = ((w & 3) * 3 + i) * 64 + lane, row = P / 12, pos = P - row * 12;
    int pc = pos - 2 * ((row >> 3) & 1); if (pc < 0) pc += 12;
    vw[i] = row * 3072 + j * 192 + pc * 16 - i * 1024;
  }
  const unsigned wdst = lds0 + WS_OFF + (w < 4 ? 0 : WG_U) + (w & 3) * 3072;
  auto w_m0 = [&](int t) { set_m0(wdst + (t & 1) * WG_SLOT); };
  auto w_so = [&](int t) { return (unsigned)(rot10(t, off) * (64 * 3072)); };
  const int rank = ((b >> 3) - 16) * 8 + (b & 7);
  const int unit = rank * 8 + w;
  const bool job = unit < N_UNITS;
  const int row0 = 1024 + rank * 8 + w;
  rowstat_issue(a, row0, lds0 + SCR_ROW + w * 1280, lane);
#pragma unroll
  for (int t = 0; t < 2; ++t) {
    w_m0(t);
    const unsigned so = w_so(t);
    asm volatile("s_nop 0");
#pragma unroll
    for (int i = 0; i < 3; ++i) dma16_i(i, vw[i], rw, so);
    x_tile(rx, lds0, t, off, w, vx);
  }
  x_tile(rx, lds0, 2, off, w, vx);
  {
    const unsigned row = 80 * p + (w & 3) * 20 + (lane >> 1);
    touch_line(lds0 + SCR_DUMMY, lane < 40 ? row * 3072 + ((j * 192) & ~127) + (lane & 1) * 128 : OOB, rw, 0);
    relayout_touch(a, unit, lds0 + SCR_DUMMY, lane);
    const int l = (((b >> 3) - 16) * 8 + w) * 40 + lane, r = l / 10, c = l - r * 10, st = r >> 5;
    touch_line(lds0 + SCR_DUMMY, lane < 40 ? (unsigned)((((st >> 2) * 12 + (st & 3)) * 32 + (r & 31)) * 1280 + c * 128) : OOB, mk_rsrc(a.wd, 1536 * 1280), 0);
  }
  unsigned xa[2], wa[3], ca[2];
#pragma unroll
  for (int s = 0; s < 2; ++s) xa[s] = (rq * 64 + r16) * 128 + (((4 * s + q) ^ (r16 >> 1)) * 16);
#pragma unroll
  for (int c = 0; c < 3; ++c) { int pos = 3 * ch + c + (q & 1); if (pos >= 6) pos -= 6; wa[c] = WS_OFF + (8 * q + (r16 >> 2)) * 192 + pos * 32 + (r16 & 3) * 8; }
#pragma unroll
  for (int i = 0; i < 2; ++i) {
    const int g = (rq + 4 * i) % 6, c = g >> 1, im = g & 1; int pos = 3 * ch + c + (q & 1); if (pos >= 6) pos -= 6;
    ca[i] = WS_OFF + im * WG_U + (8 * q + (r16 >> 2)) * 192 + pos * 32 + (r16 & 3) * 8;
  }
  f32x4 acc[6][4];
#pragma unroll
  for (int g = 0; g < 6; ++g)
#pragma unroll
    for (int rt = 0; rt < 4; ++rt) acc[g][rt] = (f32x4){0.f, 0.f, 0.f, 0.f};
  f32x4 cs[2] = {{0.f, 0.f, 0.f, 0.f}, {0.f, 0.f, 0.f, 0.f}};
  bool gok = true;
  bf16x8 XF[2][4], WF[4];
  auto rdX = [&](int t, int s, int rt) { XF[s][rt] = ld_b128(sm + xa[s] + (t % 3) * XS_SLOT + rt * 2048); };
  auto rdW = [&](int G) {
    const int t = G / 12, s = (G % 12) / 6, g = G % 6, c = g >> 1, im = g & 1;
    const LAS char* pp = sm + wa[c] + (t & 1) * WG_SLOT + im * WG_U + s * 6144;
    WF[G % 4] = cat(ld_tr(pp), ld_tr(pp + 768));
  };
  unsigned wso = 0, xso = 0;
  auto dma_pre = [&](int tb, int k) {
    if (k == 0 && tb + 2 < 10) { w_m0(tb + 2); wso = w_so(tb + 2); }
    if (k == 3 && tb + 3 < 10) { x_m0(lds0, tb + 3, w); xso = rot10(tb + 3, off) * 128; }
  };
  auto dma_piece = [&](int tb, int k) {
    if (k < 3) { if (tb + 2 < 10) dma16_i(k, vw[k], rw, wso); }
    else if (tb + 3 < 10) dma16_i(k - 3, vx[k - 3], rx, xso);
  };
  WAIT_VM(21);
  tag = *(const LAS unsigned*)(sm + SCR_TAG + w * 256) + 1u;
  { RowStat rs; rowstat_a(rs, sm + SCR_ROW + w * 1280, lane); rowstat_b(rs); rowstat_c(rs, lane); rowstat_d(a, rs, row0, lane, tag); }
  auto colsum = [&](int t) {
    if (cs_owner(p, t)) {
      bf16x8 cf[4];
#pragma unroll
      for (int i = 0; i < 2; ++i)
#pragma unroll
        for (int s2 = 0; s2 < 2; ++s2) { const LAS char* pp = sm + ca[i] + (t & 1) * WG_SLOT + s2 * 6144; cf[2 * i + s2] = cat(ld_tr(pp), ld_tr(pp + 768)); }
      const bf16x8 on = ones_if(true);
      cs[0] = mfma16(cf[1], on, mfma16(cf[0], on, cs[0])); cs[1] = mfma16(cf[3], on, mfma16(cf[2], on, cs[1]));
      if (t >= 2) {
#pragma unroll
        for (int i = 0; i < 2; ++i) { const int g2 = rq + 4 * i, c = g2 >> 1, im = g2 & 1; cs_publish(a, 1, j, p, (i == 0 || rq < 2) ? im * 96 + (3 * ch + c) * 16 : -1, cs[i], lane, tag); }
      }
    }
  };
  WAIT_BAR(15);
  if (w >= 4) __builtin_amdgcn_s_setprio(1);
  colsum(0);
#pragma unroll
  for (int rt = 0; rt < 4; ++rt) rdX(0, 0, rt);
  rdW(0); rdW(1); rdW(2);
  SB();
#pragma unroll
  for (int t = 0; t < 10; ++t) {
#pragma unroll
    for (int s = 0; s < 2; ++s) {
#pragma unroll
      for (int g = 0; g < 6; ++g) {
        const int G = 12 * t + 6 * s + g;
        if (s == 1 && g == 3 && t < 9) {
          if (t == 0) { WAIT_BAR(8); }
          else if (t < 8) { WAIT_BAR(4); } else { WAIT_BAR(0); }
          if (t + 1 < 5) colsum(t + 1);
          if (t == 7 && job) relayout_issue(a, unit, lds0 + XS_SLOT + w * 4096, lane);
          SB();
        }
        const bf16x8 Wc = WF[G % 4];
        acc[g][0] = mfma16(Wc, XF[s][0], acc[g][0]);
        acc[g][1] = mfma16(Wc, XF[s][1], acc[g][1]);
        if (G + 3 < 120) rdW(G + 3);
        if (s == 1 && g >= 3 && t < 9) dma_pre(t, g - 3);
        if (s == 0 && g <= 3 && t > 0) dma_pre(t - 1, g + 3);
        SB();
        acc[g][2] = mfma16(Wc, XF[s][2], acc[g][2]);
        acc[g][3] = mfma16(Wc, XF[s][3], acc[g][3]);
        if (s == 0 && g <= 3) rdX(t, 1, g);
        if (s == 1 && (g == 3 || g == 4) && t < 9) { rdX(t + 1, 0, 2 * (g - 3)); rdX(t + 1, 0, 2 * (g - 3) + 1); }
        if (s == 1 && g >= 3 && t < 9) dma_piece(t, g - 3);
        if (s == 0 && g <= 3 && t > 0) dma_piece(t - 1, g + 3);
        if (t == 8 && s == 0 && g < 2) gather_piece<192>(a, 1, j, p, lds0, lane, w + 8 * g);
        if (t == 9 && s == 1 && g == 0) gok = gather_fast<192>(tid, sm, tag, (LAS float*)(sm + GL_MISC), (LAS float*)(sm + GL_MISC) + 192);
        if (t == 9 && s == 0 && g == 1 && job) relayout_finish(a, unit, sm + XS_SLOT + w * 4096, lane);
        SB();
      }
    }
  }
  LAS float* csl = (LAS float*)(sm + GL_MISC);
  LAS float* stl = csl + 192;
  __builtin_amdgcn_s_setprio(0);
  if (!__all(gok)) gather_finish<192>(a, 1, j, p, tid, sm, tag, csl, stl);
  WAIT_BAR(0);
  if (tid == 0) __hip_atomic_store((GAS unsigned*)(a.ws + WS_RL) + rank, tag, RLX_AGENT);
  const rsrc_t rafr = mk_rsrc(a.ws + WS_AFR, (unsigned)AFR_BYTES);
  f32x4 cgv[3], cuv[3];
#pragma unroll
  for (int c = 0; c < 3; ++c) { cgv[c] = *(LAS f32x4*)(csl + (3 * ch + c) * 16 + 4 * q); cuv[c] = *(LAS f32x4*)(csl + 96 + (3 * ch + c) * 16 + 4 * q); }
  f32x2 st4[4];
#pragma unroll
  for (int rt = 0; rt < 4; ++rt) st4[rt] = *(LAS f32x2*)(stl + (rq * 64 + rt * 16 + r16) * 2);
  LDS_BAR();
#pragma unroll
  for (int rt = 0; rt < 4; ++rt) {
#pragma unroll
    for (int i = 0; i < 3; ++i) if (rt * 3 + i < 10) g2_preload_piece(a, lds0, b, lane, w * 10 + rt * 3 + i);
    SB();
    const f32x2 st = {st4[rt][0], st4[rt][0] * st4[rt][1]};
    const float ga = -LOG2E * st[0], gb = -LOG2E * st[1];
    unsigned hp[3][2];
#pragma unroll
    for (int c = 0; c < 3; ++c) {
      float hv[4];
#pragma unroll
      for (int e = 0; e < 4; ++e) {
        const float ex = __builtin_amdgcn_exp2f(ga * acc[2 * c][rt][e] + gb * cgv[c][e]);
        const float u = st[0] * acc[2 * c + 1][rt][e] + st[1] * cuv[c][e];
        hv[e] = u * __builtin_amdgcn_rcpf(1.f + ex);
      }
      hp[c][0] = pk2(hv[0], hv[1]); hp[c][1] = pk2(hv[2], hv[3]);
    }
    const unsigned base = (unsigned)(((p * 16 + rq * 4 + rt) * KS + 20 + 3 * j) * 1024 + lane * 16);
    if (ch == 0) {
      __builtin_amdgcn_raw_buffer_store_b128((u32x4){hp[0][0], hp[0][1], hp[1][0], hp[1][1]}, rafr, base, 0, SC1);
      __builtin_amdgcn_raw_buffer_store_b64((u32x2){hp[2][0], hp[2][1]}, rafr, base + 1024, 0, SC1);
    } else {
      __builtin_amdgcn_raw_buffer_store_b64((u32x2){hp[0][0], hp[0][1]}, rafr, base + 1024 + 8, 0, SC1);
      __builtin_amdgcn_raw_buffer_store_b128((u32x4){hp[1][0], hp[1][1], hp[2][0], hp[2][1]}, rafr, base + 2048, 0, SC1);
    }
  }
}

DI void dual_softmax4(f32x4 (&d1)[4], f32x4 (&d2)[4], const float (&sc)[4], float (&L)[4][10]) {
  float z[4][5], mx[4], mn[4], s1[4], s2[4];
  mfma_fence(d1, d2);
#pragma unroll
  for (int r = 0; r < 4; ++r) {
#pragma unroll
    for (int i = 0; i < 5; ++i) { z[r][i] = (i < 4 ? d1[r][i & 3] : d2[r][0]) * sc[r]; PIN1(z[r][i]); }
    mx[r] = mx3(mx3i(z[r][0], z[r][1], z[r][2]), z[r][3], z[r][4]);
    mn[r] = mn3(mn3i(z[r][0], z[r][1], z[r][2]), z[r][3], z[r][4]);
  }
#pragma unroll
  for (int r = 0; r < 4; ++r) { mx[r] = q_max(mx[r]); mn[r] = q_min(mn[r]); }
#pragma unroll
  for (int r = 0; r < 4; ++r) {
#pragma unroll
    for (int i = 0; i < 5; ++i) { L[r][i] = __builtin_amdgcn_exp2f(z[r][i] - mx[r]); L[r][5 + i] = __builtin_amdgcn_exp2f(mn[r] - z[r][i]); }
    s1[r] = ((L[r][0] + L[r][1]) + (L[r][2] + L[r][3])) + L[r][4];
    s2[r] = ((L[r][5] + L[r][6]) + (L[r][7] + L[r][8])) + L[r][9];
  }
#pragma unroll
  for (int r = 0; r < 4; ++r) { s1[r] = __builtin_amdgcn_rcpf(q_add(s1[r])); s2[r] = __builtin_amdgcn_rcpf(q_add(s2[r])); }
#pragma unroll
  for (int r = 0; r < 4; ++r)
#pragma unroll
    for (int i = 0; i < 5; ++i) { L[r][i] *= s1[r]; L[r][5 + i] *= s2[r]; }
}

DI void qkv_body(const Args& a, char* smem, int p, int j, int w, int lane, int b, int off, unsigned& tag) {
  const LAS char* sm = (const LAS char*)smem;
  LAS char* smw = (LAS char*)smem;
  const unsigned lds0 = (unsigned)(uintptr_t)sm;
  const int r16 = lane & 15, q = lane >> 4, tid = w * 64 + lane;
  const int rq = w >> 1, hd = w & 1;
  const rsrc_t rx = mk_rsrc(a.x + (size_t)p * 256 * 640, 256 * 1280);
  const rsrc_t rw = mk_rsrc(a.wqkv, 640 * 3840);
  unsigned vx[4];
  x_offsets(w, lane, vx);
  unsigned vw;
  { const int row = 8 * w + (lane >> 4), rot = (lane >> 4) + 4 * (w & 1); int pi = ((lane & 15) - 2 * rot) & 15; if (pi > 14) pi = 14; const int part = pi / 5, pc = pi - part * 5; vw = row * 3840 + part * 1280 + j * 80 + pc * 16; }
  const unsigned vw1 = vw + 4 * 3840 - 1024;
  auto w_m0 = [&](int t) { set_m0(lds0 + WS_OFF + (t & 1) * WQ_SLOT + 2 * w * 1024); };
  auto w_so = [&](int t) { return (unsigned)(rot10(t, off) * (64 * 3840)); };
  rowstat_issue(a, b * 8 + w, lds0 + SCR_ROW + w * 1280, lane);
  { const rsrc_t rp = mk_rsrc(a.wphi + (2 * j + (w >> 2)) * 400, 800); if (lane < 50) dma16(lds0 + QK_RAW + w * 800, lane * 16, rp, 0); }
#pragma unroll
  for (int t = 0; t < 2; ++t) {
    w_m0(t);
    const unsigned so = w_so(t);
    asm volatile("s_nop 0");
    dma16_off<0>(vw, rw, so); dma16_off<1024>(vw1, rw, so);
    x_tile(rx, lds0, t, off, w, vx);
  }
  x_tile(rx, lds0, 2, off, w, vx);
  {
    const int l6 = lane / 6, sub = lane - l6 * 6;
    const unsigned row = 80 * p + 10 * w + l6, o = (sub >> 1) * 1280 + j * 80 + (sub & 1) * 79;
    touch_line(lds0 + SCR_DUMMY, lane < 60 ? row * 3840 + (o & ~3u) : OOB, rw, 0);
  }
  unsigned xa[2], wa[4], ca;
#pragma unroll
  for (int s = 0; s < 2; ++s) xa[s] = (rq * 64 + r16) * 128 + (((4 * s + q) ^ (r16 >> 1)) * 16);
  {
    const int pp = r16 & 3, rot = (r16 >> 2) + 4 * (q & 1);
    const int g0 = 5 * hd + pp;
    const int g1 = pp == 0 ? 5 * hd + 4 : 10 + 5 * hd + pp - 1;
    const int g2 = pp < 2 ? 10 + 5 * hd + 3 + pp : 20 + 5 * hd + pp - 2;
    const int g3 = 20 + 5 * hd + 2 + (pp < 3 ? pp : 2);
    const int gg[4] = {g0, g1, g2, g3};
#pragma unroll
    for (int t = 0; t < 4; ++t) wa[t] = WS_OFF + (8 * q + (r16 >> 2)) * 256 + ((gg[t] + 4 * rot) & 31) * 8;
    const int gq = rq == 0 ? g0 : rq == 1 ? g1 : rq == 2 ? g2 : g3;
    ca = WS_OFF + (8 * q + (r16 >> 2)) * 256 + ((gq + 4 * rot) & 31) * 8;
  }
  f32x4 acc[4][4];
#pragma unroll
  for (int g = 0; g < 4; ++g)
#pragma unroll
    for (int rt = 0; rt < 4; ++rt) acc[g][rt] = (f32x4){0.f, 0.f, 0.f, 0.f};
  f32x4 cs = {0.f, 0.f, 0.f, 0.f};
  bool gok = true;
  bf16x8 XF[2][4], WF[4];
  auto rdX = [&](int t, int s, int rt) { XF[s][rt] = ld_b128(sm + xa[s] + (t % 3) * XS_SLOT + rt * 2048); };
  auto rdW = [&](int G) {
    const int t = G / 8, s = (G % 8) / 4, g = G % 4;
    const LAS char* pp = sm + wa[g] + (t & 1) * WQ_SLOT + s * 8192;
    WF[G % 4] = cat(ld_tr(pp), ld_tr(pp + 1024));
  };
  unsigned wso = 0, xso = 0;
  auto dma_pre = [&](int tb, int k) {
    if (k == 0 && tb + 2 < 10) { w_m0(tb + 2); wso = w_so(tb + 2); }
    if (k == 2 && tb + 3 < 10) { x_m0(lds0, tb + 3, w); xso = rot10(tb + 3, off) * 128; }
  };
  auto dma_piece = [&](int tb, int k) {
    if (k == 0) { if (tb + 2 < 10) dma16_off<0>(vw, rw, wso); }
    else if (k == 1) { if (tb + 2 < 10) dma16_off<1024>(vw1, rw, wso); }
    else if (tb + 3 < 10) dma16_i(k - 2, vx[k - 2], rx, xso);
  };
  WAIT_VM(17);
  tag = *(const LAS unsigned*)(sm + SCR_TAG + w * 256) + 1u;
  { RowStat rs; rowstat_a(rs, sm + SCR_ROW + w * 1280, lane); rowstat_b(rs); rowstat_c(rs, lane); rowstat_d(a, rs, b * 8 + w, lane, tag); }
  unsigned pe[8];
  auto colsum = [&](int t) {
    if (cs_owner(p, t)) {
      bf16x8 cf[2];
#pragma unroll
      for (int s2 = 0; s2 < 2; ++s2) { const LAS char* pp = sm + ca + (t & 1) * WQ_SLOT + s2 * 8192; cf[s2] = cat(ld_tr(pp), ld_tr(pp + 1024)); }
      const bf16x8 on = ones_if(true);
      cs = mfma16(cf[1], on, mfma16(cf[0], on, cs));
      if (t >= 2) cs_publish(a, 0, j, p, hd * 64 + rq * 16, cs, lane, tag);
    }
  };
  WAIT_BAR(12);
  if (w >= 4) __builtin_amdgcn_s_setprio(1);
  colsum(0);
#pragma unroll
  for (int rt = 0; rt < 4; ++rt) rdX(0, 0, rt);
  rdW(0); rdW(1); rdW(2);
  SB();
#pragma unroll
  for (int t = 0; t < 10; ++t) {
#pragma unroll
    for (int s = 0; s < 2; ++s) {
#pragma unroll
      for (int g = 0; g < 4; ++g) {
        const int G = 8 * t + 4 * s + g;
        if (s == 1 && g == 1 && t < 9) {
          if (t == 0) { WAIT_BAR(6); }
          else if (t < 8) { WAIT_BAR(4); } else { WAIT_BAR(0); }
          if (t + 1 < 5) colsum(t + 1);
          SB();
        }
        const bf16x8 Wc = WF[G % 4];
        acc[g][0] = mfma16(Wc, XF[s][0], acc[g][0]);
        acc[g][1] = mfma16(Wc, XF[s][1], acc[g][1]);
        if (G + 3 < 80) rdW(G + 3);
        if (s == 1 && g >= 1 && t < 9) dma_pre(t, g - 1);
        if (s == 0 && g <= 2 && t > 0) dma_pre(t - 1, g + 3);
        SB();
        acc[g][2] = mfma16(Wc, XF[s][2], acc[g][2]);
        acc[g][3] = mfma16(Wc, XF[s][3], acc[g][3]);
        if (s == 0 && g < 2) { rdX(t, 1, 2 * g); rdX(t, 1, 2 * g + 1); }
        if (s == 1 && (g == 1 || g == 2) && t < 9) { rdX(t + 1, 0, 2 * (g - 1)); rdX(t + 1, 0, 2 * (g - 1) + 1); }
        if (s == 1 && g >= 1 && t < 9) dma_piece(t, g - 1);
        if (s == 0 && g <= 2 && t > 0) dma_piece(t - 1, g + 3);
        if (t == 5) {
          if (s == 0 && g == 0) {
            const int isk = (w >> 1) & 1, ft = w & 1;
            const int qq = r16 >> 2, ee = r16 & 3;
            const bool fok = ft == 0 || ee == 0;
            const int f = 5 * qq + (ft == 0 ? ee : 4);
            const LAS uint16_t* wp = (const LAS uint16_t*)(sm + QK_RAW + w * 800);
#pragma unroll
            for (int x = 0; x < 8; ++x) {
              int d;
              if (isk == 0) d = x < 4 ? 4 * q + x : (q == 0 ? 16 + x - 4 : -1);
              else d = x < 4 ? (q >= 1 ? 4 * (q - 1) + x : -1) : (q < 2 ? 12 + 4 * q + x - 4 : -1);
              const bool ok = fok && d >= 0;
              const unsigned v = wp[ok ? d * 20 + f : 0];
              pe[x] = ok ? v : 0u;
            }
          }
          if (s == 1 && g == 0) *(LAS u32x4*)(smw + QK_PHI + w * 1024 + lane * 16) = (u32x4){pe[0] | (pe[1] << 16), pe[2] | (pe[3] << 16), pe[4] | (pe[5] << 16), pe[6] | (pe[7] << 16)};
        }
        if (t == 8 && s == 0 && g < 2) gather_piece<128>(a, 0, j, p, lds0, lane, w + 8 * g);
        if (t == 9 && s == 0 && g == 1) gok = gather_fast<128>(tid, sm, tag, (LAS float*)(smw + QK_MISC), (LAS float*)(smw + QK_MISC) + 128);
        SB();
      }
    }
  }
  LAS float* csl = (LAS float*)(smw + QK_MISC);
  LAS float* stl = csl + 128;
  __builtin_amdgcn_s_setprio(0);
  if (!__all(gok)) gather_finish<128>(a, 0, j, p, tid, sm, tag, csl, stl);
  LDS_BAR();
  constexpr int IROW = 280, ONES = 256 * IROW, KVT = ONES + 64;
  static_assert(KVT + 8192 <= G2_FLAG_C, "images overlap the staged tiles");
  f32x4 c4[4];
#pragma unroll
  for (int t = 0; t < 4; ++t) c4[t] = *(LAS f32x4*)(csl + hd * 64 + t * 16 + 4 * q);
  const f32x4 z4 = {0.f, 0.f, 0.f, 0.f};
  const bf16x8 Aq1 = ld_b128(sm + QK_PHI + (4 * hd + 0) * 1024 + lane * 16), Aq2 = ld_b128(sm + QK_PHI + (4 * hd + 1) * 1024 + lane * 16);
  const bf16x8 Ak1 = ld_b128(sm + QK_PHI + (4 * hd + 2) * 1024 + lane * 16), Ak2 = ld_b128(sm + QK_PHI + (4 * hd + 3) * 1024 + lane * 16);
  f32x2 st4[4];
#pragma unroll
  for (int rt = 0; rt < 4; ++rt) st4[rt] = *(LAS f32x2*)(stl + (rq * 64 + rt * 16 + r16) * 2);
  unsigned pkd[4][4][2];
  float sc[4];
#pragma unroll
  for (int rt = 0; rt < 4; ++rt) {
    sc[rt] = st4[rt][0] * LOG2E;
    const float s2 = q >= 2 ? st4[rt][0] : 1.f;
    const f32x4 v1 = acc[1][rt] + st4[rt][1] * c4[1], v2 = (acc[2][rt] + st4[rt][1] * c4[2]) * s2, v3 = (acc[3][rt] + st4[rt][1] * c4[3]) * st4[rt][0];
    pkd[rt][1][0] = pk2(v1[0], v1[1]); pkd[rt][1][1] = pk2(v1[2], v1[3]);
    pkd[rt][2][0] = pk2(v2[0], v2[1]); pkd[rt][2][1] = pk2(v2[2], v2[3]);
    pkd[rt][3][0] = pk2(v3[0], v3[1]); pkd[rt][3][1] = pk2(v3[2], v3[3]);
  }
  {
    f32x4 dk1[4], dk2[4];
#pragma unroll
    for (int rt = 0; rt < 4; ++rt) {
      const bf16x8 Xb = __builtin_bit_cast(bf16x8, (u32x4){pkd[rt][1][0], pkd[rt][1][1], pkd[rt][2][0], pkd[rt][2][1]});
      dk1[rt] = mfma16(Ak1, Xb, z4); dk2[rt] = mfma16(Ak2, Xb, z4);
    }
    float L[4][10];
    dual_softmax4(dk1, dk2, sc, L);
    LAS char* row0 = smw + (rq * 64 + r16) * IROW;
    const int va = q >= 2 ? 192 + hd * 40 + (q - 2) * 8 : 272, vb = q < 3 ? 192 + hd * 40 + (q + 2) * 8 : 272;
#pragma unroll
    for (int rt = 0; rt < 4; ++rt) {
      LAS char* ir = row0 + rt * 16 * IROW;
      LAS char* kp = ir + hd * 96 + q * 24;
      *(LAS u32x2*)(kp) = (u32x2){pk2(L[rt][0], L[rt][1]), pk2(L[rt][2], L[rt][3])};
      *(LAS u32x2*)(kp + 8) = (u32x2){pk2(L[rt][4], L[rt][5]), pk2(L[rt][6], L[rt][7])};
      *(LAS u32x2*)(kp + 16) = (u32x2){pk2(L[rt][8], L[rt][9]), 0u};
      *(LAS u32x2*)(ir + va) = (u32x2){pkd[rt][2][0], pkd[rt][2][1]};
      *(LAS u32x2*)(ir + vb) = (u32x2){pkd[rt][3][0], pkd[rt][3][1]};
    }
  }
  if (tid == 0) *(LAS u32x2*)(smw + ONES) = (u32x2){0x00003f80u, 0u};
  if (tid < 256) *(LAS u32x2*)(smw + KVT + (tid >> 2) * 128 + (tid & 3) * 32 + 24) = (u32x2){0u, 0u};
  LDS_BAR();
  const rsrc_t rkv = mk_rsrc(a.ws + WS_KVP + (size_t)j * 8 * KVP_STRIDE, 8 * KVP_STRIDE);
  if (w < 6) {
    const int uh = w / 3, mt = w - uh * 3;
    f32x4 d0 = z4, d1 = z4;
#pragma unroll
    for (int ks = 0; ks < 8; ++ks) {
      bf16x4 av[2], b0[2], b1[2];
#pragma unroll
      for (int u = 0; u < 2; ++u) {
        const LAS char* rp = sm + (32 * ks + 8 * q + 4 * u + (r16 >> 2)) * IROW;
        av[u] = ld_tr(rp + uh * 96 + mt * 32 + (r16 & 3) * 8);
        b0[u] = ld_tr(rp + 192 + uh * 40 + (r16 & 3) * 8);
        b1[u] = ld_tr((r16 & 3) == 0 ? rp + 192 + uh * 40 + 32 : sm + ONES);
      }
      const bf16x8 A = cat(av[0], av[1]);
      d0 = mfma16(A, cat(b0[0], b0[1]), d0);
      d1 = mfma16(A, cat(b1[0], b1[1]), d1);
    }
    mfma_fence(d0, d1);
    const unsigned base = (unsigned)(p * KVP_STRIDE + (uh * 3 + mt) * 1344);
    __builtin_amdgcn_raw_buffer_store_b128((u32x4){pk2(d0[0], d0[1]), tag, pk2(d0[2], d0[3]), tag}, rkv, base + lane * 16, 0, SC1);
    __builtin_amdgcn_raw_buffer_store_b128((u32x4){pk2(d1[0], d1[1]), tag, pk2(d1[2], d1[3]), tag}, rkv, r16 < 5 ? base + 1024 + (q * 5 + r16) * 16 : OOB, 0, SC1);
  }
  auto preload = [&](int i0, int i1) {
#pragma unroll
    for (int i = i0; i < i1; ++i) g2_preload_piece(a, lds0, b, lane, w * 10 + i);
    SB();
  };
  preload(0, 3);
  u32x4 Fq0[4]; unsigned Fq1[4];
  {
    f32x4 dq1[4], dq2[4];
#pragma unroll
    for (int rt = 0; rt < 4; ++rt) {
      const f32x4 v0 = acc[0][rt] + st4[rt][1] * c4[0];
      const bf16x8 Xa = __builtin_bit_cast(bf16x8, (u32x4){pk2(v0[0], v0[1]), pk2(v0[2], v0[3]), pkd[rt][1][0], pkd[rt][1][1]});
      dq1[rt] = mfma16(Aq1, Xa, z4); dq2[rt] = mfma16(Aq2, Xa, z4);
    }
    SB(); preload(3, 7);
    float L[4][10];
    dual_softmax4(dq1, dq2, sc, L);
    SB(); preload(7, 10);
#pragma unroll
    for (int rt = 0; rt < 4; ++rt) {
      Fq0[rt] = (u32x4){pk2(L[rt][0], L[rt][1]), pk2(L[rt][2], L[rt][3]), pk2(L[rt][4], L[rt][5]), pk2(L[rt][6], L[rt][7])};
      Fq1[rt] = pk2(L[rt][8], L[rt][9]);
    }
  }
  if (tid < 504) {
    u32x4 v[8];
    for (unsigned spins = 0; spins < SPIN_CAP; ++spins) {
      bool ok = true;
#pragma unroll
      for (int k = 0; k < 8; ++k) v[k] = __builtin_amdgcn_raw_buffer_load_b128(rkv, (unsigned)(tid * 16), k * KVP_STRIDE, SC1);
#pragma unroll
      for (int k = 0; k < 8; ++k) ok &= (v[k][1] == tag) & (v[k][3] == tag);
      if (__all(ok)) break;
      __builtin_amdgcn_s_sleep(1);
    }
    f32x4 s = z4;
#pragma unroll
    for (int k = 0; k < 8; ++k) { const unsigned lo = v[k][0], hi = v[k][2]; s += (f32x4){uf(lo << 16), uf(lo & 0xffff0000u), uf(hi << 16), uf(hi & 0xffff0000u)}; }
    const int un = tid / 84, o = tid - un * 84, uh = un / 3, mt = un - uh * 3;
    int qq, dc;
    if (o < 64) { qq = o >> 4; dc = o & 15; } else { const int o2 = o - 64; qq = o2 / 5; dc = 16 + (o2 - qq * 5); }
    const int phi = 16 * mt + 4 * qq, lq = phi / 12, m = phi - 12 * lq;
    *(LAS u32x2*)(smw + KVT + (uh * 32 + dc) * 128 + lq * 32 + m * 2) = (u32x2){pk2(s[0], s[1]), pk2(s[2], s[3])};
  }
  LDS_BAR();
  {
    bf16x8 At[2][2];
#pragma unroll
    for (int mt = 0; mt < 2; ++mt)
#pragma unroll
      for (int s = 0; s < 2; ++s) At[mt][s] = ld_b128(sm + KVT + (hd * 32 + mt * 16 + r16) * 128 + q * 32 + s * 16);
    const rsrc_t rafr = mk_rsrc(a.ws + WS_AFR, (unsigned)AFR_BYTES);
    const int H = 2 * j + hd;
    const int sA = 20 * H + 4 * q, sB = 20 * H + 16;
    const unsigned oA = (unsigned)((sA >> 5) * 1024 + (((sA & 31) >> 3) * 16 + r16) * 16 + ((sA >> 2) & 1) * 8);
    const unsigned oB = (unsigned)((sB >> 5) * 1024 + (((sB & 31) >> 3) * 16 + r16) * 16 + ((sB >> 2) & 1) * 8);
    f32x4 od0[4], od1[4];
#pragma unroll
    for (int rt = 0; rt < 4; ++rt) {
      const bf16x8 B0 = __builtin_bit_cast(bf16x8, Fq0[rt]), B1 = __builtin_bit_cast(bf16x8, (u32x4){Fq1[rt], 0u, 0u, 0u});
      od0[rt] = mfma16(At[0][1], B1, mfma16(At[0][0], B0, z4));
      od1[rt] = mfma16(At[1][1], B1, mfma16(At[1][0], B0, z4));
    }
    mfma_fence(od0, od1);
#pragma unroll
    for (int rt = 0; rt < 4; ++rt) {
      const f32x4 d0 = od0[rt], d1 = od1[rt];
      const float inv = __builtin_amdgcn_rcpf(bcast_q1(d1[0]) + 1e-6f);
      const unsigned base = (unsigned)((p * 16 + rq * 4 + rt) * KS * 1024);
      __builtin_amdgcn_raw_buffer_store_b64((u32x2){pk2(d0[0] * inv, d0[1] * inv), pk2(d0[2] * inv, d0[3] * inv)}, rafr, base + oA, 0, SC1);
      if (q == 0) __builtin_amdgcn_raw_buffer_store_b64((u32x2){pk2(d1[0] * inv, d1[1] * inv), pk2(d1[2] * inv, d1[3] * inv)}, rafr, base + oB, 0, SC1);
    }
  }
}

constexpr int NH = 12, NO = 5, NS2 = NH + NO;
constexpr int NPRE = 4;
constexpr int G2_PRE = 81920;
constexpr int G2_FLAG = G2_PRE - 64;
DI int g2_ks(int w, int s) { return s >= NH ? 5 * w + (s - NH) : 20 + NH * w + s; }
DI void g2_preload_piece(const Args& a, unsigned lds0, int b, int lane, int f) {
  const int ct80 = (b >> 3) & 7, tile = f / 5, i = f - tile * 5, w4 = tile / NPRE, s = tile - w4 * NPRE;
  const rsrc_t r = mk_rsrc(a.wd, 1536 * 1280);
  const int P = i * 64 + lane, row = P / 10, pc = P - row * 10;
  dma16(lds0 + G2_PRE + f * 1024, (unsigned)(row * 1280 + pc * 16), r, (unsigned)((NH * w4 + s) * 32 * 1280 + ct80 * 160));
}
DI void gemm2_body(const Args& a, char* smem, int w, int lane, int b, unsigned tag) {
  const int tid = w * 64 + lane;
  const int xcd = b & 7, idx = b >> 3;
  const int rt64 = xcd * 4 + (idx >> 3), ct80 = idx & 7;
  const LAS char* sm = (const LAS char*)smem;
  volatile LAS unsigned* gflag = (volatile LAS unsigned*)(smem + G2_FLAG);
  u32x4 xr[3];
  LAS float* red = (LAS float*)smem;
  f32x4 acc[5][4];
  if (w < 4) {
    const rsrc_t ra = mk_rsrc(a.ws + WS_AFR + (size_t)rt64 * 4 * KS * 1024, 4 * KS * 1024);
    const rsrc_t rb = mk_rsrc(a.ws + WS_BFR + (size_t)ct80 * 5 * KS * 1024, 5 * KS * 1024);
    constexpr int DA = 4, DB = 3;
    u32x4 A[DA][4], B[DB][5];
    bf16x8 BL[3];
    const unsigned voff = lane * 16;
    auto ldB = [&](int s, int c) { B[s % DB][c] = __builtin_amdgcn_raw_buffer_load_b128(rb, voff, (unsigned)(g2_ks(w, s) + c * KS) * 1024u, SC1); };
    auto ldA = [&](int s, int r) { A[s % DA][r] = __builtin_amdgcn_raw_buffer_load_b128(ra, voff, (unsigned)(g2_ks(w, s) + r * KS) * 1024u, SC1); };
    const LAS char* pre = sm + G2_PRE + w * NPRE * 5120 + (4 * (lane >> 4) + ((lane & 15) >> 2)) * 160 + (lane & 3) * 8;
    auto rdB = [&](int f) { const LAS char* pp = pre + (f / 5) * 5120 + (f % 5) * 32; BL[f % 3] = cat(ld_tr(pp), ld_tr(pp + 16 * 160)); };
#pragma unroll
    for (int s = 0; s < DA - 1; ++s)
#pragma unroll
      for (int r = 0; r < 4; ++r) ldA(s, r);
    rdB(0); rdB(1);
#pragma unroll
    for (int c = 0; c < 5; ++c)
#pragma unroll
      for (int r = 0; r < 4; ++r) acc[c][r] = (f32x4){0.f, 0.f, 0.f, 0.f};
    SB();
#pragma unroll
    for (int s = 0; s < NS2; ++s) {
      if (s == NH - DA + 1) {
        for (unsigned spins = 0; spins < SPIN_CAP * 8; ++spins) { if (*gflag == 1u) break; __builtin_amdgcn_s_sleep(1); }
        SB();
      }
#pragma unroll
      for (int c = 0; c < 5; ++c) {
        if (s * 5 + c + 2 < NPRE * 5) rdB(s * 5 + c + 2);
        bf16x8 Bc;
        if (s < NPRE) Bc = BL[(s * 5 + c) % 3];
        else Bc = __builtin_bit_cast(bf16x8, B[s % DB][c]);
        acc[c][0] = mfma16(Bc, __builtin_bit_cast(bf16x8, A[s % DA][0]), acc[c][0]);
        acc[c][1] = mfma16(Bc, __builtin_bit_cast(bf16x8, A[s % DA][1]), acc[c][1]);
        SB();
        if (c < 4 && s - 1 + DA < NS2) ldA(s - 1 + DA, c);
        SB();
        acc[c][2] = mfma16(Bc, __builtin_bit_cast(bf16x8, A[s % DA][2]), acc[c][2]);
        acc[c][3] = mfma16(Bc, __builtin_bit_cast(bf16x8, A[s % DA][3]), acc[c][3]);
        SB();
        if (s + DB >= NPRE && s + DB < NS2) ldB(s + DB, c);
        SB();
      }
    }
  } else {
    const rsrc_t ra = mk_rsrc(a.ws + WS_AFR + (size_t)rt64 * 4 * KS * 1024, 4 * KS * 1024);
    const unsigned dummy = (unsigned)(uintptr_t)sm + G2_FLAG - 256;
    if (w < 7) { const int l = ct80 * 192 + (w - 4) * 64 + lane, rt = l / 384, rem = l - rt * 384; touch_line(dummy, (unsigned)((rt * KS + 20) * 1024 + rem * 128), ra, 0); }
#pragma unroll
    for (int i = 0; i < 3; ++i) {
      const int it = tid - 256 + 256 * i;
      if (it < 640) { const int row = it / 10, g = it - row * 10; xr[i] = *(const u32x4*)(a.x + (size_t)(rt64 * 64 + row) * 640 + ct80 * 80 + g * 8); }
    }
    if (w == 7) {
      for (unsigned spins = 0; spins < SPIN_CAP; ++spins) { if (panel_ok(a, 0, xcd, lane, tag)) break; __builtin_amdgcn_s_sleep(1); }
#pragma unroll
      for (int i = 0; i < 2; ++i) { const int l = ct80 * 80 + i * 64 + lane, rt = l / 160, rem = l - rt * 160; touch_line(dummy, i * 64 + lane < 80 ? (unsigned)(rt * KS * 1024 + rem * 128) : OOB, ra, 0); }
      if (lane == 0) *gflag = 1u;
      if (b == 0) {
        unsigned fq[4];
        flags_load<4>(a, 0, lane, fq);
        flags_wait<4>(a, 0, lane, fq, tag);
        if (lane == 0) __hip_atomic_store((GAS unsigned*)(a.ws + WS_EPOCH), tag, RLX_AGENT);
      }
    }
  }
  LDS_BAR();
  if (w < 4) {
#pragma unroll
    for (int c = 0; c < 5; ++c)
#pragma unroll
      for (int r = 0; r < 4; ++r) *(LAS f32x4*)(red + (w * 64 + r * 16 + (lane & 15)) * 84 + c * 16 + (lane >> 4) * 4) = acc[c][r];
  }
  LDS_BAR();
  if (w >= 4)
#pragma unroll
  for (int i = 0; i < 3; ++i) {
    const int it = tid - 256 + 256 * i;
    if (it < 640) {
      const int row = it / 10, g = it - row * 10;
      f32x4 s0 = {0.f, 0.f, 0.f, 0.f}, s1 = {0.f, 0.f, 0.f, 0.f};
#pragma unroll
      for (int k = 0; k < 4; ++k) { s0 += *(LAS f32x4*)(red + (k * 64 + row) * 84 + g * 8); s1 += *(LAS f32x4*)(red + (k * 64 + row) * 84 + g * 8 + 4); }
      u32x4 o;
#pragma unroll
      for (int e = 0; e < 4; ++e) {
        const unsigned xe = xr[i][e];
        const float v0 = e < 2 ? s0[2 * e] : s1[2 * e - 4], v1 = e < 2 ? s0[2 * e + 1] : s1[2 * e - 3];
        o[e] = pk2(uf(xe << 16) + v0, uf(xe & 0xffff0000u) + v1);
      }
      *(u32x4*)(a.out + (size_t)(rt64 * 64 + row) * 640 + ct80 * 80 + g * 8) = o;
    }
  }
}

__global__ __launch_bounds__(512, 2) void fused_kernel(Args a) {
  extern __shared__ __attribute__((aligned(16))) char smem[];
  const int tid = threadIdx.x, lane = tid & 63, w = rfl(tid >> 6);
  const int b = blockIdx.x, xcd = b & 7, idx = b >> 3;
  const int p = idx & 7, jj = (idx >> 3) & 1, j = xcd * 2 + jj;
  unsigned tag;
  dma4_sc1((unsigned)(uintptr_t)(LAS char*)smem + SCR_TAG + w * 256, 0u, mk_rsrc(a.ws + WS_EPOCH, 64));
  const int off = p >= 4 ? 5 : 0;
  if (idx < 16) qkv_body(a, smem, p, j, w, lane, b, off, tag);
  else glu_body(a, smem, p, j, w, lane, b, off, tag);
  volatile LAS unsigned* lf = (volatile LAS unsigned*)((LAS char*)smem + G2_FLAG);
  if (tid == 0) lf[0] = 0u;
  WAIT_BAR(0);
  if (tid == 0) __hip_atomic_store((GAS unsigned*)(a.ws + WS_DONE) + b * 4, tag, RLX_AGENT);
  if (w == 0)
    for (unsigned spins = 0; spins < SPIN_CAP; ++spins) { if (panel_ok(a, 1, xcd, lane, tag)) break; __builtin_amdgcn_s_sleep(1); }
  LDS_BAR();
  gemm2_body(a, smem, w, lane, b, tag);
}

extern "C" void kernel_launch(void* const* d_in, const int* in_sizes, int n_in, void* d_out, int out_size, void* d_ws, size_t ws_size, hipStream_t stream) {
  (void)in_sizes; (void)n_in; (void)out_size; (void)ws_size;
  Args a;
  a.x = (const uint16_t*)d_in[0]; a.wqkv = (const uint16_t*)d_in[1]; a.wphi = (const uint16_t*)d_in[2]; a.wo = (const uint16_t*)d_in[3];
  a.wg = (const uint16_t*)d_in[5]; a.wu = (const uint16_t*)d_in[6]; a.wd = (const uint16_t*)d_in[7]; a.ws = (char*)d_ws; a.out = (uint16_t*)d_out;
  (void)hipFuncSetAttribute((const void*)fused_kernel, hipFuncAttributeMaxDynamicSharedMemorySize, LDS_BYTES);
  fused_kernel<<<256, 512, LDS_BYTES, stream>>>(a);
}
```
